```python
import torch

def _softmax(x, axis=-1):
    return torch.softmax(x, dim=axis)
def _gelu(x):
    return torch.nn.functional.gelu(x, approximate="tanh")
def _silu(x):
    return torch.nn.functional.silu(x)
def _sigmoid(x):
    return torch.sigmoid(x)
def _layernorm(x, axis=-1, eps=1e-5):
    m = x.mean(dim=axis, keepdim=True)
    v = x.var(dim=axis, keepdim=True, unbiased=False)
    return (x - m) / torch.sqrt(v + eps)
def _scatter_add(tgt, idx, src, axis=0):
    return tgt.clone().index_add_(axis, idx, src)
def _segment_sum(src, seg, n):
    out = torch.zeros((n,) + src.shape[1:], dtype=src.dtype)
    return out.index_add_(0, seg, src)
def _pad(x, pw):
    flat = tuple(v for pair in reversed(pw) for v in pair)
    return torch.nn.functional.pad(x, flat)

kernel_name = 'concat_heads__gqa_block_cached__mha_block_cached__near_ridge'

def setup_inputs():

    _w = (lambda a: (lambda t: t.to(torch.bfloat16) if t.is_floating_point() else t)(torch.as_tensor(a)))
    def _A_setup():
        B, Sq, D = 8, 32, 512
        Hq, Hkv, Sk, Dh = 32, 8, 2048, 128
        assert Hq % Hkv == 0, (Hq, Hkv)
        return {
            "x": _w((lambda *s: torch.randn(*s, dtype=torch.bfloat16) * 0.8)(B, Sq, D)),
            "k_cache": _w((lambda *s: torch.randn(*s, dtype=torch.bfloat16) * 0.8)(B, Hkv, Sk, Dh)),
            "v_cache": _w((lambda *s: torch.randn(*s, dtype=torch.bfloat16) * 0.05)(B, Hkv, Sk, Dh)),
            "wq": _w((lambda *s: torch.randn(*s, dtype=torch.bfloat16) * 0.8)(D, Hq * Dh) * (D ** -0.5)),
            "wo": _w((lambda *s: torch.randn(*s, dtype=torch.bfloat16) * 0.05)(Hq * Dh, D) * ((Hq * Dh) ** -0.5)),
        }
    _A_in = _A_setup()

    def _B_setup():
        B, Sq, D = 8, 32, 512
        H, Sk, Dh = 32, 2048, 128
        return {
            "x": _w((lambda *s: torch.randn(*s, dtype=torch.bfloat16) * 0.8)(B, Sq, D)),
            "k_cache": _w((lambda *s: torch.randn(*s, dtype=torch.bfloat16) * 0.8)(B, H, Sk, Dh)),
            "v_cache": _w((lambda *s: torch.randn(*s, dtype=torch.bfloat16) * 0.05)(B, H, Sk, Dh)),
            "wq": _w((lambda *s: torch.randn(*s, dtype=torch.bfloat16) * 0.8)(D, H * Dh) * (D ** -0.5)),
            "wo": _w((lambda *s: torch.randn(*s, dtype=torch.bfloat16) * 0.05)(H * Dh, D) * ((H * Dh) ** -0.5)),
        }
    _B_in = _B_setup()

    return {
        "A_x": _A_in["x"],
        "A_k_cache": _A_in["k_cache"],
        "A_v_cache": _A_in["v_cache"],
        "A_wq": _A_in["wq"],
        "A_wo": _A_in["wo"],
        "B_x": _B_in["x"],
        "B_k_cache": _B_in["k_cache"],
        "B_v_cache": _B_in["v_cache"],
        "B_wq": _B_in["wq"],
        "B_wo": _B_in["wo"],
    }

def reference(A_x, A_k_cache, A_v_cache, A_wq, A_wo, B_x, B_k_cache, B_v_cache, B_wq, B_wo):
    def _A_ref(x, k_cache, v_cache, wq, wo):
        B, Sq, D = x.shape
        Hq, Dh = 32, 128
        Hkv = k_cache.shape[1]
        rep = Hq // Hkv
        q = (lambda x, *d: x.permute(*d))((x @ wq).reshape(B, Sq, Hq, Dh), 0, 2, 1, 3)
        k = (lambda x, n, axis: torch.repeat_interleave(x, n, dim=axis))(k_cache, rep, axis=1)
        v = (lambda x, n, axis: torch.repeat_interleave(x, n, dim=axis))(v_cache, rep, axis=1)
        scores = torch.einsum("bhqd,bhkd->bhqk", q, k) * (Dh ** -0.5)
        p = _softmax(scores, axis=-1)
        o = torch.einsum("bhqk,bhkd->bhqd", p, v)
        return (lambda x, *d: x.permute(*d))(o, 0, 2, 1, 3).reshape(B, Sq, Hq * Dh) @ wo

    def _B_ref(x, k_cache, v_cache, wq, wo):
        B, Sq, D = x.shape
        H, Dh = 32, 128
        q = (lambda x, *d: x.permute(*d))((x @ wq).reshape(B, Sq, H, Dh), 0, 2, 1, 3)
        scores = torch.einsum("bhqd,bhkd->bhqk", q, k_cache) * (Dh ** -0.5)
        p = _softmax(scores, axis=-1)
        o = torch.einsum("bhqk,bhkd->bhqd", p, v_cache)
        return (lambda x, *d: x.permute(*d))(o, 0, 2, 1, 3).reshape(B, Sq, H * Dh) @ wo

    a = _A_ref(A_x, A_k_cache, A_v_cache, A_wq, A_wo)
    b = _B_ref(B_x, B_k_cache, B_v_cache, B_wq, B_wo)
    return torch.concatenate([a, b], axis=-1)
```

```cpp
#include <hip/hip_runtime.h>
#include <hip/hip_bf16.h>
#include <stdint.h>

#define DI __device__ __forceinline__
#define LAS __attribute__((address_space(3)))

typedef __attribute__((ext_vector_type(8))) __bf16 bf16x8;
typedef __attribute__((ext_vector_type(4))) __bf16 bf16x4;
typedef __attribute__((ext_vector_type(2))) __bf16 bf16x2;
typedef __attribute__((ext_vector_type(2))) float f32x2;
typedef __attribute__((ext_vector_type(4))) float f32x4;
typedef __attribute__((ext_vector_type(16))) float f32x16;
typedef __attribute__((ext_vector_type(4))) unsigned u32x4;
typedef __attribute__((ext_vector_type(2))) unsigned u32x2;
typedef __amdgpu_buffer_rsrc_t rsrc_t;

constexpr int NB = 8, SQ = 32, DM = 512, NH = 32, SK = 2048, DH = 128;
constexpr int NT = 16;
constexpr unsigned RING = 0, SCR = 131072;
constexpr unsigned LDS1 = 163840;
constexpr float SC2 = 0.08838834764831845f * 1.4426950408889634f;

#define NT_B " nt"
#define NT_A ""

DI unsigned rfl(unsigned x) { return __builtin_amdgcn_readfirstlane(x); }
DI rsrc_t mk_rsrc(const void* p, unsigned bytes) { return __builtin_amdgcn_make_buffer_rsrc((void*)p, 0, bytes, 0x00020000); }
DI unsigned cvtpk(float lo, float hi) { f32x2 v = {lo, hi}; bf16x2 b = __builtin_convertvector(v, bf16x2); return __builtin_bit_cast(unsigned, b); }
DI void bar() { asm volatile("s_waitcnt lgkmcnt(0)\n\ts_barrier" ::: "memory"); }

#define DMA_TILE(NTS) asm volatile( \
    "s_waitcnt lgkmcnt(0)\n\t" \
    "s_mov_b32 m0, %0\n\ts_nop 0\n\t" \
    "buffer_load_dwordx4 %5, %4, %2 offen" NTS " lds\n\t" \
    "buffer_load_dwordx4 %6, %4, %2 offen offset:1024" NTS " lds\n\t" \
    "buffer_load_dwordx4 %7, %4, %2 offen offset:2048" NTS " lds\n\t" \
    "buffer_load_dwordx4 %8, %4, %2 offen offset:3072" NTS " lds\n\t" \
    "s_mov_b32 m0, %1\n\ts_nop 0\n\t" \
    "buffer_load_dwordx4 %5, %4, %3 offen" NTS " lds\n\t" \
    "buffer_load_dwordx4 %6, %4, %3 offen offset:1024" NTS " lds\n\t" \
    "buffer_load_dwordx4 %7, %4, %3 offen offset:2048" NTS " lds\n\t" \
    "buffer_load_dwordx4 %8, %4, %3 offen offset:3072" NTS " lds" \
    :: "s"(lds), "s"(lds + 4096u), "s"(soff), "s"(soff + 4096u), "s"(srd), "v"(vo[0]), "v"(vo[1]), "v"(vo[2]), "v"(vo[3]) : "memory")
DI void dma_tile(int grp, unsigned lds, rsrc_t srd, unsigned soff, const unsigned (&vo)[4]) {
  if (grp) DMA_TILE(NT_A); else DMA_TILE(NT_B);
}
DI u32x4 bload(rsrc_t srd, unsigned voff, unsigned soff) {
  u32x4 r; asm volatile("buffer_load_dwordx4 %0, %1, %2, %3 offen" : "=v"(r) : "v"(voff), "s"(srd), "s"(soff) : "memory"); return r;
}

__global__ __launch_bounds__(512, 2) void attn_kernel(const void* __restrict__ xA, const void* __restrict__ kA, const void* __restrict__ vA, const void* __restrict__ wqA,
                                                      const void* __restrict__ xB, const void* __restrict__ kB, const void* __restrict__ vB, const void* __restrict__ wqB,
                                                      u32x4* __restrict__ ows) {
  extern __shared__ __attribute__((aligned(16))) char smem_[];
  LAS char* smem = (LAS char*)smem_;
  const unsigned lds0 = (unsigned)(uintptr_t)smem;
  const int lane = threadIdx.x & 63;
  const int wave = rfl(threadIdx.x >> 6), grp = wave >> 2, wv = wave & 3;
  const int g8 = blockIdx.x & 7, jj = blockIdx.x >> 3, b = jj >> 2, head = 4 * g8 + (jj & 3);
  const size_t kvoff = (size_t)(grp ? b * 8 + g8 : b * 32 + head) * (SK * DH * 2);
  const rsrc_t srdK = mk_rsrc((const char*)(grp ? kA : kB) + kvoff, SK * DH * 2);
  const rsrc_t srdV = mk_rsrc((const char*)(grp ? vA : vB) + kvoff, SK * DH * 2);
  const rsrc_t srdW = mk_rsrc(grp ? wqA : wqB, DM * NH * DH * 2);
  const rsrc_t srdX = mk_rsrc((const char*)(grp ? xA : xB) + (size_t)b * (SQ * DM * 2), SQ * DM * 2);
  const unsigned kslot = RING + wave * 16384u, vslot = kslot + 8192u;
  unsigned vo[4];
#pragma unroll
  for (int i = 0; i < 4; ++i) vo[i] = (lane >> 4) * 256 + 16 * ((lane & 15) ^ (((lane >> 4) << 2) | i));

  bf16x8 qf[8];
  {
    const int c = lane & 15, g = lane >> 4;
    u32x4 xr[4][2], wr[4][8];
    const unsigned xv0 = (c + 16 * (wv >> 1)) * 1024 + g * 16, xv1 = (c + 16 * ((wv >> 1) ^ 1)) * 1024 + g * 16;
    const unsigned wvo = g * 65536 + 16 * (c ^ (2 * (wv & 1)));
#pragma unroll
    for (int ks = 0; ks < 4; ++ks) {
      xr[ks][0] = bload(srdX, xv0, wv * 256 + ks * 64);
      xr[ks][1] = bload(srdX, xv1, wv * 256 + ks * 64);
#pragma unroll
      for (int i = 0; i < 8; ++i) wr[ks][i] = bload(srdW, wvo, (wv * 128 + ks * 32 + i) * 8192 + head * 256);
    }
    dma_tile(grp, lds0 + kslot, srdK, wv * 8192u, vo);
    dma_tile(grp, lds0 + vslot, srdV, wv * 8192u, vo);
    f32x4 acc[8][2];
#pragma unroll
    for (int ks = 0; ks < 4; ++ks) {
#define QWAIT(N) asm volatile("s_waitcnt vmcnt(" #N ")" : "+v"(xr[ks][0]), "+v"(xr[ks][1]), "+v"(wr[ks][0]), "+v"(wr[ks][1]), "+v"(wr[ks][2]), "+v"(wr[ks][3]), \
                              "+v"(wr[ks][4]), "+v"(wr[ks][5]), "+v"(wr[ks][6]), "+v"(wr[ks][7]) :: "memory")
      __builtin_amdgcn_sched_barrier(0);
      if (ks == 0) QWAIT(46); else if (ks == 1) QWAIT(36); else if (ks == 2) QWAIT(26); else QWAIT(16);
#undef QWAIT
#pragma unroll
      for (int j = 0; j < 8; ++j) {
        u32x4 f;
#pragma unroll
        for (int m = 0; m < 4; ++m) f[m] = __builtin_amdgcn_perm(wr[ks][2 * m + 1][j >> 1], wr[ks][2 * m][j >> 1], (j & 1) ? 0x07060302u : 0x05040100u);
#pragma unroll
        for (int qb = 0; qb < 2; ++qb)
          acc[j][qb] = __builtin_amdgcn_mfma_f32_16x16x32_bf16(__builtin_bit_cast(bf16x8, f), __builtin_bit_cast(bf16x8, xr[ks][qb]), ks ? acc[j][qb] : f32x4{0.f, 0.f, 0.f, 0.f}, 0, 0, 0);
      }
    }
    LAS f32x4* mine = (LAS f32x4*)(smem + SCR + wave * 4096) + lane;
#pragma unroll
    for (int r = 1; r < 4; ++r) {
      const int qb = r >> 1, rp = r & 1;
#pragma unroll
      for (int u = 0; u < 4; ++u) mine[u * 64] = f32x4{acc[2 * u][qb][2 * rp], acc[2 * u][qb][2 * rp + 1], acc[2 * u + 1][qb][2 * rp], acc[2 * u + 1][qb][2 * rp + 1]};
      bar();
      LAS f32x4* other = (LAS f32x4*)(smem + SCR + (wave ^ r) * 4096) + lane;
#pragma unroll
      for (int u = 0; u < 4; ++u) { const f32x4 v = other[u * 64]; acc[2 * u][0][0] += v[0]; acc[2 * u][0][1] += v[1]; acc[2 * u + 1][0][0] += v[2]; acc[2 * u + 1][0][1] += v[3]; }
      bar();
    }
    LAS char* qimg = smem + SCR + grp * 8192;
    const int q = c + 16 * (wv >> 1);
#pragma unroll
    for (int e = 0; e < 2; ++e) {
      const int ch = 4 * g + 2 * (wv & 1) + e;
      *(LAS u32x4*)(qimg + q * 256 + 16 * (ch ^ c)) = u32x4{cvtpk(acc[0][0][e], acc[1][0][e]), cvtpk(acc[2][0][e], acc[3][0][e]), cvtpk(acc[4][0][e], acc[5][0][e]), cvtpk(acc[6][0][e], acc[7][0][e])};
    }
    bar();
#pragma unroll
    for (int s = 0; s < 8; ++s) qf[s] = *(LAS bf16x8*)(qimg + (lane & 31) * 256 + 16 * ((2 * s + (lane >> 5)) ^ (lane & 15)));
  }

  const int r32 = lane & 31, hi = lane >> 5;
  const unsigned kaddr = kslot + 256 * r32 + 16 * (hi ^ (((r32 & 3) << 2) | ((r32 >> 2) & 3)));
  const int blk = (lane >> 4) & 1, q4 = (lane & 15) >> 2, p4 = lane & 3;
  const unsigned vaddr = vslot + 256 * (4 * hi + q4) + 64 * q4 + 16 * ((2 * blk + (p4 & 1)) ^ hi) + 8 * (p4 >> 1);
  f32x16 o[4] = {};
  float m = -__builtin_inff(), l = 0.f;
#pragma unroll 1
  for (int t = 0; t < NT; ++t) {
    const bool more = t + 1 < NT;
    const unsigned soffn = (unsigned)((t + 1) * 4 + wv) * 8192u;
    asm volatile("s_waitcnt vmcnt(8)" ::: "memory");
    bf16x8 kf[8];
#pragma unroll
    for (int s = 0; s < 8; ++s) kf[s] = *(LAS bf16x8*)(smem + (kaddr ^ (32u * s)));
    if (more) dma_tile(grp, lds0 + kslot, srdK, soffn, vo);
    f32x16 sc = {};
#pragma unroll
    for (int s = 0; s < 8; ++s) sc = __builtin_amdgcn_mfma_f32_32x32x16_bf16(kf[s], qf[s], sc, 0, 0, 0);
    float mx = sc[0];
#pragma unroll
    for (int i = 1; i < 16; ++i) mx = fmaxf(mx, sc[i]);
    { auto rr = __builtin_amdgcn_permlane32_swap(__float_as_uint(mx), __float_as_uint(mx), false, false); mx = fmaxf(__uint_as_float(rr[0]), __uint_as_float(rr[1])); }
    const float mn = fmaxf(m, mx), alpha = __builtin_amdgcn_exp2f((m - mn) * SC2), nm = -mn * SC2;
    m = mn;
    float ps = 0.f;
#pragma unroll
    for (int i = 0; i < 16; ++i) { sc[i] = __builtin_amdgcn_exp2f(fmaf(sc[i], SC2, nm)); ps += sc[i]; }
    l = fmaf(l, alpha, ps);
    bf16x8 pf[2];
#pragma unroll
    for (int s = 0; s < 2; ++s) pf[s] = __builtin_bit_cast(bf16x8, u32x4{cvtpk(sc[8 * s], sc[8 * s + 1]), cvtpk(sc[8 * s + 2], sc[8 * s + 3]), cvtpk(sc[8 * s + 4], sc[8 * s + 5]), cvtpk(sc[8 * s + 6], sc[8 * s + 7])});
#pragma unroll
    for (int c = 0; c < 4; ++c) o[c] *= alpha;
    if (more) asm volatile("s_waitcnt vmcnt(8)" ::: "memory"); else asm volatile("s_waitcnt vmcnt(0)" ::: "memory");
    bf16x8 vf[4][2];
#pragma unroll
    for (int c = 0; c < 4; ++c)
#pragma unroll
      for (int s = 0; s < 2; ++s) {
        const bf16x4 lo = __builtin_amdgcn_ds_read_tr16_b64_v4bf16((LAS bf16x4*)(smem + ((vaddr ^ (64u * c)) + 4096u * s)));
        const bf16x4 up = __builtin_amdgcn_ds_read_tr16_b64_v4bf16((LAS bf16x4*)(smem + ((vaddr ^ (64u * c) ^ 32u) + 4096u * s + 2048u)));
        vf[c][s] = __builtin_shufflevector(lo, up, 0, 1, 2, 3, 4, 5, 6, 7);
      }
    if (more) dma_tile(grp, lds0 + vslot, srdV, soffn, vo);
#pragma unroll
    for (int s = 0; s < 2; ++s)
#pragma unroll
      for (int c = 0; c < 4; ++c) o[c] = __builtin_amdgcn_mfma_f32_32x32x16_bf16(vf[c][s], pf[s], o[c], 0, 0, 0);
  }

  { auto rr = __builtin_amdgcn_permlane32_swap(__float_as_uint(l), __float_as_uint(l), false, false); l = __uint_as_float(rr[0]) + __uint_as_float(rr[1]); }
  LAS f32x2* ml = (LAS f32x2*)(smem + SCR + 16384);
  if (hi == 0) ml[wave * 32 + r32] = f32x2{m, l};
  bar();
  {
    f32x2 v[4]; float ms = -__builtin_inff(), L = 0.f;
#pragma unroll
    for (int w = 0; w < 4; ++w) { v[w] = ml[(grp * 4 + w) * 32 + r32]; ms = fmaxf(ms, v[w][0]); }
#pragma unroll
    for (int w = 0; w < 4; ++w) L = fmaf(__builtin_amdgcn_exp2f((v[w][0] - ms) * SC2), v[w][1], L);
    const float f = __builtin_amdgcn_exp2f((m - ms) * SC2) / L;
    LAS f32x4* mine = (LAS f32x4*)(smem + kslot) + lane;
#pragma unroll
    for (int c = 0; c < 4; ++c)
#pragma unroll
      for (int u = 0; u < 4; ++u) mine[(c * 4 + u) * 64] = f32x4{o[c][4 * u] * f, o[c][4 * u + 1] * f, o[c][4 * u + 2] * f, o[c][4 * u + 3] * f};
  }
  bar();
  {
    f32x4 a[4] = {};
#pragma unroll
    for (int w = 0; w < 4; ++w) {
      LAS f32x4* src = (LAS f32x4*)(smem + RING + (grp * 4 + w) * 16384 + wv * 4096) + lane;
#pragma unroll
      for (int u = 0; u < 4; ++u) a[u] += src[u * 64];
    }
    u32x4* dst = ows + ((size_t)(((grp ^ 1) * NB + b) * NH + head) * 8 + 2 * wv) * 64 + lane;
    dst[0]  = u32x4{cvtpk(a[0][0], a[0][1]), cvtpk(a[0][2], a[0][3]), cvtpk(a[1][0], a[1][1]), cvtpk(a[1][2], a[1][3])};
    dst[64] = u32x4{cvtpk(a[2][0], a[2][1]), cvtpk(a[2][2], a[2][3]), cvtpk(a[3][0], a[3][1]), cvtpk(a[3][2], a[3][3])};
  }
}

__global__ __launch_bounds__(512, 2) void oproj_kernel(const void* __restrict__ woA, const void* __restrict__ woB, const u32x4* __restrict__ ows, __hip_bfloat16* __restrict__ out) {
  extern __shared__ __attribute__((aligned(16))) char smem_[];
  LAS char* smem = (LAS char*)smem_;
  const unsigned lds0 = (unsigned)(uintptr_t)smem;
  const int lane = threadIdx.x & 63, wave = rfl(threadIdx.x >> 6);
  const int g8 = blockIdx.x & 7, jj = blockIdx.x >> 3;
  const int br = g8 >> 2, b = 4 * ((g8 >> 1) & 1) + (jj >> 3), cb = 8 * (g8 & 1) + (jj & 7);
  const rsrc_t srdW = mk_rsrc(br ? woB : woA, NH * DH * DM * 2);
  const rsrc_t srdO = mk_rsrc(ows + (size_t)(br * NB + b) * NH * 8 * 64, NH * 8 * 1024);
  const unsigned slot = wave * 16384u;
  const unsigned wvo = (lane >> 2) * 1024 + (lane & 3) * 16;
  const unsigned wso = wave * 512 * 1024 + cb * 64;
#define DMA_W(R) _Pragma("unroll") for (int i = 0; i < 16; ++i) \
    asm volatile("s_mov_b32 m0, %0\n\ts_nop 0\n\tbuffer_load_dwordx4 %1, %2, %3 offen lds" :: "s"(lds0 + slot + 1024u * i), "v"(wvo), "s"(srdW), "s"(wso + ((R) * 16 + i) * 16384u) : "memory")
  DMA_W(0);
  u32x4 of[32];
#pragma unroll
  for (int s = 0; s < 32; ++s) of[s] = bload(srdO, lane * 16, (wave * 32 + s) * 1024);
  const int hi = lane >> 5, blk = (lane >> 4) & 1, q4 = (lane & 15) >> 2, p4 = lane & 3;
  const unsigned waddr = slot + 64 * (8 * hi + q4) + 32 * blk + 8 * p4;
  bf16x8 wf[16];
  asm volatile("s_waitcnt vmcnt(32)" ::: "memory");
#define RD_W() _Pragma("unroll") for (int s = 0; s < 16; ++s) { \
    const bf16x4 lo = __builtin_amdgcn_ds_read_tr16_b64_v4bf16((LAS bf16x4*)(smem + waddr + 1024u * s)); \
    const bf16x4 up = __builtin_amdgcn_ds_read_tr16_b64_v4bf16((LAS bf16x4*)(smem + waddr + 1024u * s + 256u)); \
    wf[s] = __builtin_shufflevector(lo, up, 0, 1, 2, 3, 4, 5, 6, 7); }
  RD_W();
  asm volatile("s_waitcnt lgkmcnt(0)" ::: "memory");
  DMA_W(1);
  f32x16 acc = {};
  asm volatile("s_waitcnt vmcnt(32)" : "+v"(of[0]), "+v"(of[1]), "+v"(of[2]), "+v"(of[3]), "+v"(of[4]), "+v"(of[5]), "+v"(of[6]), "+v"(of[7]),
               "+v"(of[8]), "+v"(of[9]), "+v"(of[10]), "+v"(of[11]), "+v"(of[12]), "+v"(of[13]), "+v"(of[14]), "+v"(of[15]) :: "memory");
#pragma unroll
  for (int s = 0; s < 16; ++s) acc = __builtin_amdgcn_mfma_f32_32x32x16_bf16(wf[s], __builtin_bit_cast(bf16x8, of[s]), acc, 0, 0, 0);
  asm volatile("s_waitcnt vmcnt(0)" : "+v"(of[16]), "+v"(of[17]), "+v"(of[18]), "+v"(of[19]), "+v"(of[20]), "+v"(of[21]), "+v"(of[22]), "+v"(of[23]),
               "+v"(of[24]), "+v"(of[25]), "+v"(of[26]), "+v"(of[27]), "+v"(of[28]), "+v"(of[29]), "+v"(of[30]), "+v"(of[31]) :: "memory");
  RD_W();
#pragma unroll
  for (int s = 0; s < 16; ++s) acc = __builtin_amdgcn_mfma_f32_32x32x16_bf16(wf[s], __builtin_bit_cast(bf16x8, of[16 + s]), acc, 0, 0, 0);
  LAS f32x4* red = (LAS f32x4*)(smem + 131072);
#pragma unroll
  for (int u = 0; u < 4; ++u) red[(wave * 4 + u) * 64 + lane] = f32x4{acc[4 * u], acc[4 * u + 1], acc[4 * u + 2], acc[4 * u + 3]};
  bar();
  if (wave < 4) {
    f32x4 a = red[wave * 64 + lane];
#pragma unroll
    for (int w = 1; w < 8; ++w) a += red[(w * 4 + wave) * 64 + lane];
    *(u32x2*)(out + (size_t)(b * SQ + (lane & 31)) * 1024 + br * 512 + cb * 32 + 8 * wave + 4 * hi) = u32x2{cvtpk(a[0], a[1]), cvtpk(a[2], a[3])};
  }
}

extern "C" void kernel_launch(void* const* d_in, const int* in_sizes, int n_in, void* d_out, int out_size, void* d_ws, size_t ws_size, hipStream_t stream) {
  (void)in_sizes; (void)n_in; (void)out_size; (void)ws_size;
  (void)hipFuncSetAttribute((const void*)attn_kernel, hipFuncAttributeMaxDynamicSharedMemorySize, LDS1);
  (void)hipFuncSetAttribute((const void*)oproj_kernel, hipFuncAttributeMaxDynamicSharedMemorySize, LDS1);
  u32x4* ows = (u32x4*)d_ws;
  hipLaunchKernelGGL(attn_kernel, dim3(256), dim3(512), LDS1, stream, d_in[0], d_in[1], d_in[2], d_in[3], d_in[5], d_in[6], d_in[7], d_in[8], ows);
  hipLaunchKernelGGL(oproj_kernel, dim3(256), dim3(512), LDS1, stream, d_in[4], d_in[9], (const u32x4*)ows, (__hip_bfloat16*)d_out);
}
```

```cpp
#define POLS 0xC
#include <hip/hip_runtime.h>
#include <hip/hip_bf16.h>
#include <stdint.h>

#define DI __device__ __forceinline__
#define LAS __attribute__((address_space(3)))

typedef __attribute__((ext_vector_type(8))) __bf16 bf16x8;
typedef __attribute__((ext_vector_type(4))) __bf16 bf16x4;
typedef __attribute__((ext_vector_type(2))) __bf16 bf16x2;
typedef __attribute__((ext_vector_type(2))) float f32x2;
typedef __attribute__((ext_vector_type(4))) float f32x4;
typedef __attribute__((ext_vector_type(16))) float f32x16;
typedef __attribute__((ext_vector_type(4))) unsigned u32x4;
typedef __attribute__((ext_vector_type(2))) unsigned u32x2;
typedef __amdgpu_buffer_rsrc_t rsrc_t;

constexpr int NB = 8, SQ = 32, DM = 512, NH = 32, SK = 2048, DH = 128;
constexpr int NT = 16;
constexpr unsigned RING = 0, SCR = 131072;
constexpr unsigned LDS1 = 163840;
constexpr float SC2 = 0.08838834764831845f * 1.4426950408889634f;

#define STAMP(k)
#define TAKE(k)
#define VB_BMIN 0

DI unsigned rfl(unsigned x) { return __builtin_amdgcn_readfirstlane(x); }
DI rsrc_t mk_rsrc(const void* p, unsigned bytes) { return __builtin_amdgcn_make_buffer_rsrc((void*)p, 0, bytes, 0x00020000); }
DI unsigned cvtpk(float lo, float hi) { f32x2 v = {lo, hi}; bf16x2 b = __builtin_convertvector(v, bf16x2); return __builtin_bit_cast(unsigned, b); }
DI void bar() { asm volatile("s_waitcnt lgkmcnt(0)\n\ts_barrier" ::: "memory"); }

#define DMA_TILE(NTS) asm volatile( \
    "s_waitcnt lgkmcnt(0)\n\t" \
    "s_mov_b32 m0, %0\n\ts_nop 0\n\t" \
    "buffer_load_dwordx4 %5, %4, %2 offen" NTS " lds\n\t" \
    "buffer_load_dwordx4 %6, %4, %2 offen offset:1024" NTS " lds\n\t" \
    "buffer_load_dwordx4 %7, %4, %2 offen offset:2048" NTS " lds\n\t" \
    "buffer_load_dwordx4 %8, %4, %2 offen offset:3072" NTS " lds\n\t" \
    "s_mov_b32 m0, %1\n\ts_nop 0\n\t" \
    "buffer_load_dwordx4 %5, %4, %3 offen" NTS " lds\n\t" \
    "buffer_load_dwordx4 %6, %4, %3 offen offset:1024" NTS " lds\n\t" \
    "buffer_load_dwordx4 %7, %4, %3 offen offset:2048" NTS " lds\n\t" \
    "buffer_load_dwordx4 %8, %4, %3 offen offset:3072" NTS " lds" \
    :: "s"(lds), "s"(lds + 4096u), "s"(soff), "s"(soff + 4096u), "s"(srd), "v"(vo[0]), "v"(vo[1]), "v"(vo[2]), "v"(vo[3]) : "memory")
DI void dma_tile(int nt, unsigned lds, rsrc_t srd, unsigned soff, const unsigned (&vo)[4]) {
  if (nt) DMA_TILE(" nt"); else DMA_TILE("");
}
DI u32x4 bload(rsrc_t srd, unsigned voff, unsigned soff) {
  u32x4 r; asm volatile("buffer_load_dwordx4 %0, %1, %2, %3 offen" : "=v"(r) : "v"(voff), "s"(srd), "s"(soff) : "memory"); return r;
}

__global__ __launch_bounds__(512, 2) void attn_kernel(const void* __restrict__ xA, const void* __restrict__ kA, const void* __restrict__ vA, const void* __restrict__ wqA,
                                                      const void* __restrict__ xB, const void* __restrict__ kB, const void* __restrict__ vB, const void* __restrict__ wqB,
                                                      u32x4* __restrict__ ows) {
  extern __shared__ __attribute__((aligned(16))) char smem_[];
  LAS char* smem = (LAS char*)smem_;
  const unsigned lds0 = (unsigned)(uintptr_t)smem;
  const int lane = threadIdx.x & 63;
  const int wave = rfl(threadIdx.x >> 6), grp = wave >> 2, wv = wave & 3;
  TAKE(0);
  const int g8 = blockIdx.x & 7, jj = blockIdx.x >> 3, b = jj >> 2, head = 4 * g8 + (jj & 3);
  const size_t kvoff = (size_t)(grp ? b * 8 + g8 : b * 32 + head) * (SK * DH * 2);
  const rsrc_t srdK = mk_rsrc((const char*)(grp ? kA : kB) + kvoff, SK * DH * 2);
  const rsrc_t srdV = mk_rsrc((const char*)(grp ? vA : vB) + kvoff, SK * DH * 2);
  const rsrc_t srdW = mk_rsrc(grp ? wqA : wqB, DM * NH * DH * 2);
  const rsrc_t srdX = mk_rsrc((const char*)(grp ? xA : xB) + (size_t)b * (SQ * DM * 2), SQ * DM * 2);
  const int ntk = grp ? (POLS & 1) : ((POLS >> 2) & 1), ntv = grp ? ((POLS >> 1) & 1) : (((POLS >> 3) & 1) | (b < VB_BMIN));
  const unsigned kslot = RING + wave * 16384u, vslot = kslot + 8192u;
  unsigned vo[4];
#pragma unroll
  for (int i = 0; i < 4; ++i) vo[i] = (lane >> 4) * 256 + 16 * ((lane & 15) ^ (((lane >> 4) << 2) | i));

  bf16x8 qf[8];
  {
    const int c = lane & 15, g = lane >> 4;
    u32x4 xr[4][2], wr[4][8];
    const unsigned xv0 = (c + 16 * (wv >> 1)) * 1024 + g * 16, xv1 = (c + 16 * ((wv >> 1) ^ 1)) * 1024 + g * 16;
    const unsigned wvo = g * 65536 + 16 * (c ^ (2 * (wv & 1)));
#pragma unroll
    for (int ks = 0; ks < 4; ++ks) {
      xr[ks][0] = bload(srdX, xv0, wv * 256 + ks * 64);
      xr[ks][1] = bload(srdX, xv1, wv * 256 + ks * 64);
#pragma unroll
      for (int i = 0; i < 8; ++i) wr[ks][i] = bload(srdW, wvo, (wv * 128 + ks * 32 + i) * 8192 + head * 256);
    }
    dma_tile(ntk, lds0 + kslot, srdK, wv * 8192u, vo);
    dma_tile(ntv, lds0 + vslot, srdV, wv * 8192u, vo);
    f32x4 acc[8][2];
#pragma unroll
    for (int ks = 0; ks < 4; ++ks) {
#define QWAIT(N) asm volatile("s_waitcnt vmcnt(" #N ")" : "+v"(xr[ks][0]), "+v"(xr[ks][1]), "+v"(wr[ks][0]), "+v"(wr[ks][1]), "+v"(wr[ks][2]), "+v"(wr[ks][3]), \
                              "+v"(wr[ks][4]), "+v"(wr[ks][5]), "+v"(wr[ks][6]), "+v"(wr[ks][7]) :: "memory")
      __builtin_amdgcn_sched_barrier(0);
      if (ks == 0) QWAIT(46); else if (ks == 1) QWAIT(36); else if (ks == 2) QWAIT(26); else QWAIT(16);
#undef QWAIT
#pragma unroll
      for (int j = 0; j < 8; ++j) {
        u32x4 f;
#pragma unroll
        for (int m = 0; m < 4; ++m) f[m] = __builtin_amdgcn_perm(wr[ks][2 * m + 1][j >> 1], wr[ks][2 * m][j >> 1], (j & 1) ? 0x07060302u : 0x05040100u);
#pragma unroll
        for (int qb = 0; qb < 2; ++qb)
          acc[j][qb] = __builtin_amdgcn_mfma_f32_16x16x32_bf16(__builtin_bit_cast(bf16x8, f), __builtin_bit_cast(bf16x8, xr[ks][qb]), ks ? acc[j][qb] : f32x4{0.f, 0.f, 0.f, 0.f}, 0, 0, 0);
      }
    }
    TAKE(1);
    LAS f32x4* mine = (LAS f32x4*)(smem + SCR + wave * 4096) + lane;
#pragma unroll
    for (int r = 1; r < 4; ++r) {
      const int qb = r >> 1, rp = r & 1;
#pragma unroll
      for (int u = 0; u < 4; ++u) mine[u * 64] = f32x4{acc[2 * u][qb][2 * rp], acc[2 * u][qb][2 * rp + 1], acc[2 * u + 1][qb][2 * rp], acc[2 * u + 1][qb][2 * rp + 1]};
      bar();
      LAS f32x4* other = (LAS f32x4*)(smem + SCR + (wave ^ r) * 4096) + lane;
#pragma unroll
      for (int u = 0; u < 4; ++u) { const f32x4 v = other[u * 64]; acc[2 * u][0][0] += v[0]; acc[2 * u][0][1] += v[1]; acc[2 * u + 1][0][0] += v[2]; acc[2 * u + 1][0][1] += v[3]; }
      bar();
    }
    LAS char* qimg = smem + SCR + grp * 8192;
    const int q = c + 16 * (wv >> 1);
#pragma unroll
    for (int e = 0; e < 2; ++e) {
      const int ch = 4 * g + 2 * (wv & 1) + e;
      *(LAS u32x4*)(qimg + q * 256 + 16 * (ch ^ c)) = u32x4{cvtpk(acc[0][0][e], acc[1][0][e]), cvtpk(acc[2][0][e], acc[3][0][e]), cvtpk(acc[4][0][e], acc[5][0][e]), cvtpk(acc[6][0][e], acc[7][0][e])};
    }
    bar();
#pragma unroll
    for (int s = 0; s < 8; ++s) qf[s] = *(LAS bf16x8*)(qimg + (lane & 31) * 256 + 16 * ((2 * s + (lane >> 5)) ^ (lane & 15)));
  }

  TAKE(2);
  const int r32 = lane & 31, hi = lane >> 5;
  const unsigned kaddr = kslot + 256 * r32 + 16 * (hi ^ (((r32 & 3) << 2) | ((r32 >> 2) & 3)));
  const int blk = (lane >> 4) & 1, q4 = (lane & 15) >> 2, p4 = lane & 3;
  const unsigned vaddr = vslot + 256 * (4 * hi + q4) + 64 * q4 + 16 * ((2 * blk + (p4 & 1)) ^ hi) + 8 * (p4 >> 1);
  f32x16 o[4] = {};
  float m = -__builtin_inff(), l = 0.f;
#pragma unroll 1
  for (int t = 0; t < NT; ++t) {
    const bool more = t + 1 < NT;
    const unsigned soffn = (unsigned)((t + 1) * 4 + wv) * 8192u;
    asm volatile("s_waitcnt vmcnt(8)" ::: "memory");
    bf16x8 kf[8];
#pragma unroll
    for (int s = 0; s < 8; ++s) kf[s] = *(LAS bf16x8*)(smem + (kaddr ^ (32u * s)));
    if (more) dma_tile(ntk, lds0 + kslot, srdK, soffn, vo);
    f32x16 sc = {};
#pragma unroll
    for (int s = 0; s < 8; ++s) sc = __builtin_amdgcn_mfma_f32_32x32x16_bf16(kf[s], qf[s], sc, 0, 0, 0);
    float mx = sc[0];
#pragma unroll
    for (int i = 1; i < 16; ++i) mx = fmaxf(mx, sc[i]);
    { auto rr = __builtin_amdgcn_permlane32_swap(__float_as_uint(mx), __float_as_uint(mx), false, false); mx = fmaxf(__uint_as_float(rr[0]), __uint_as_float(rr[1])); }
    const float mn = fmaxf(m, mx), alpha = __builtin_amdgcn_exp2f((m - mn) * SC2), nm = -mn * SC2;
    m = mn;
    float ps = 0.f;
#pragma unroll
    for (int i = 0; i < 16; ++i) { sc[i] = __builtin_amdgcn_exp2f(fmaf(sc[i], SC2, nm)); ps += sc[i]; }
    l = fmaf(l, alpha, ps);
    bf16x8 pf[2];
#pragma unroll
    for (int s = 0; s < 2; ++s) pf[s] = __builtin_bit_cast(bf16x8, u32x4{cvtpk(sc[8 * s], sc[8 * s + 1]), cvtpk(sc[8 * s + 2], sc[8 * s + 3]), cvtpk(sc[8 * s + 4], sc[8 * s + 5]), cvtpk(sc[8 * s + 6], sc[8 * s + 7])});
#pragma unroll
    for (int c = 0; c < 4; ++c) o[c] *= alpha;
    if (more) asm volatile("s_waitcnt vmcnt(8)" ::: "memory"); else asm volatile("s_waitcnt vmcnt(0)" ::: "memory");
    bf16x8 vf[4][2];
#pragma unroll
    for (int c = 0; c < 4; ++c)
#pragma unroll
      for (int s = 0; s < 2; ++s) {
        const bf16x4 lo = __builtin_amdgcn_ds_read_tr16_b64_v4bf16((LAS bf16x4*)(smem + ((vaddr ^ (64u * c)) + 4096u * s)));
        const bf16x4 up = __builtin_amdgcn_ds_read_tr16_b64_v4bf16((LAS bf16x4*)(smem + ((vaddr ^ (64u * c) ^ 32u) + 4096u * s + 2048u)));
        vf[c][s] = __builtin_shufflevector(lo, up, 0, 1, 2, 3, 4, 5, 6, 7);
      }
    if (more) dma_tile(ntv, lds0 + vslot, srdV, soffn, vo);
#pragma unroll
    for (int s = 0; s < 2; ++s)
#pragma unroll
      for (int c = 0; c < 4; ++c) o[c] = __builtin_amdgcn_mfma_f32_32x32x16_bf16(vf[c][s], pf[s], o[c], 0, 0, 0);
  }

  TAKE(3);
  { auto rr = __builtin_amdgcn_permlane32_swap(__float_as_uint(l), __float_as_uint(l), false, false); l = __uint_as_float(rr[0]) + __uint_as_float(rr[1]); }
  LAS f32x2* ml = (LAS f32x2*)(smem + SCR + 16384);
  if (hi == 0) ml[wave * 32 + r32] = f32x2{m, l};
  bar();
  {
    f32x2 v[4]; float ms = -__builtin_inff(), L = 0.f;
#pragma unroll
    for (int w = 0; w < 4; ++w) { v[w] = ml[(grp * 4 + w) * 32 + r32]; ms = fmaxf(ms, v[w][0]); }
#pragma unroll
    for (int w = 0; w < 4; ++w) L = fmaf(__builtin_amdgcn_exp2f((v[w][0] - ms) * SC2), v[w][1], L);
    const float f = __builtin_amdgcn_exp2f((m - ms) * SC2) / L;
    LAS f32x4* mine = (LAS f32x4*)(smem + kslot) + lane;
#pragma unroll
    for (int c = 0; c < 4; ++c)
#pragma unroll
      for (int u = 0; u < 4; ++u) mine[(c * 4 + u) * 64] = f32x4{o[c][4 * u] * f, o[c][4 * u + 1] * f, o[c][4 * u + 2] * f, o[c][4 * u + 3] * f};
  }
  bar();
  {
    f32x4 a[4] = {};
#pragma unroll
    for (int w = 0; w < 4; ++w) {
      LAS f32x4* src = (LAS f32x4*)(smem + RING + (grp * 4 + w) * 16384 + wv * 4096) + lane;
#pragma unroll
      for (int u = 0; u < 4; ++u) a[u] += src[u * 64];
    }
    u32x4* dst = ows + ((size_t)(((grp ^ 1) * NB + b) * NH + head) * 8 + 2 * wv) * 64 + lane;
    dst[0]  = u32x4{cvtpk(a[0][0], a[0][1]), cvtpk(a[0][2], a[0][3]), cvtpk(a[1][0], a[1][1]), cvtpk(a[1][2], a[1][3])};
    dst[64] = u32x4{cvtpk(a[2][0], a[2][1]), cvtpk(a[2][2], a[2][3]), cvtpk(a[3][0], a[3][1]), cvtpk(a[3][2], a[3][3])};
  }
  TAKE(4);
  STAMP(0); STAMP(1); STAMP(2); STAMP(3); STAMP(4);
}

__global__ __launch_bounds__(512, 2) void oproj_kernel(const void* __restrict__ woA, const void* __restrict__ woB, const u32x4* __restrict__ ows, __hip_bfloat16* __restrict__ out) {
  extern __shared__ __attribute__((aligned(16))) char smem_[];
  LAS char* smem = (LAS char*)smem_;
  const unsigned lds0 = (unsigned)(uintptr_t)smem;
  const int lane = threadIdx.x & 63, wave = rfl(threadIdx.x >> 6);
  const int g8 = blockIdx.x & 7, jj = blockIdx.x >> 3;
  const int br = g8 >> 2, b = 4 * ((g8 >> 1) & 1) + (jj >> 3), cb = 8 * (g8 & 1) + (jj & 7);
  const rsrc_t srdW = mk_rsrc(br ? woB : woA, NH * DH * DM * 2);
  const rsrc_t srdO = mk_rsrc(ows + (size_t)(br * NB + b) * NH * 8 * 64, NH * 8 * 1024);
  const unsigned slot = wave * 16384u;
  const unsigned wvo = (lane >> 2) * 1024 + (lane & 3) * 16;
  const unsigned wso = wave * 512 * 1024 + cb * 64;
#define DMA_W(R) _Pragma("unroll") for (int i = 0; i < 16; ++i) \
    asm volatile("s_mov_b32 m0, %0\n\ts_nop 0\n\tbuffer_load_dwordx4 %1, %2, %3 offen lds" :: "s"(lds0 + slot + 1024u * i), "v"(wvo), "s"(srdW), "s"(wso + ((R) * 16 + i) * 16384u) : "memory")
  DMA_W(0);
  u32x4 of[32];
#pragma unroll
  for (int s = 0; s < 32; ++s) of[s] = bload(srdO, lane * 16, (wave * 32 + s) * 1024);
  const int hi = lane >> 5, blk = (lane >> 4) & 1, q4 = (lane & 15) >> 2, p4 = lane & 3;
  const unsigned waddr = slot + 64 * (8 * hi + q4) + 32 * blk + 8 * p4;
  bf16x8 wf[16];
  asm volatile("s_waitcnt vmcnt(32)" ::: "memory");
#define RD_W() _Pragma("unroll") for (int s = 0; s < 16; ++s) { \
    const bf16x4 lo = __builtin_amdgcn_ds_read_tr16_b64_v4bf16((LAS bf16x4*)(smem + waddr + 1024u * s)); \
    const bf16x4 up = __builtin_amdgcn_ds_read_tr16_b64_v4bf16((LAS bf16x4*)(smem + waddr + 1024u * s + 256u)); \
    wf[s] = __builtin_shufflevector(lo, up, 0, 1, 2, 3, 4, 5, 6, 7); }
  RD_W();
  asm volatile("s_waitcnt lgkmcnt(0)" ::: "memory");
  DMA_W(1);
  f32x16 acc = {};
  asm volatile("s_waitcnt vmcnt(32)" : "+v"(of[0]), "+v"(of[1]), "+v"(of[2]), "+v"(of[3]), "+v"(of[4]), "+v"(of[5]), "+v"(of[6]), "+v"(of[7]),
               "+v"(of[8]), "+v"(of[9]), "+v"(of[10]), "+v"(of[11]), "+v"(of[12]), "+v"(of[13]), "+v"(of[14]), "+v"(of[15]) :: "memory");
#pragma unroll
  for (int s = 0; s < 16; ++s) acc = __builtin_amdgcn_mfma_f32_32x32x16_bf16(wf[s], __builtin_bit_cast(bf16x8, of[s]), acc, 0, 0, 0);
  asm volatile("s_waitcnt vmcnt(0)" : "+v"(of[16]), "+v"(of[17]), "+v"(of[18]), "+v"(of[19]), "+v"(of[20]), "+v"(of[21]), "+v"(of[22]), "+v"(of[23]),
               "+v"(of[24]), "+v"(of[25]), "+v"(of[26]), "+v"(of[27]), "+v"(of[28]), "+v"(of[29]), "+v"(of[30]), "+v"(of[31]) :: "memory");
  RD_W();
#pragma unroll
  for (int s = 0; s < 16; ++s) acc = __builtin_amdgcn_mfma_f32_32x32x16_bf16(wf[s], __builtin_bit_cast(bf16x8, of[16 + s]), acc, 0, 0, 0);
  LAS f32x4* red = (LAS f32x4*)(smem + 131072);
#pragma unroll
  for (int u = 0; u < 4; ++u) red[(wave * 4 + u) * 64 + lane] = f32x4{acc[4 * u], acc[4 * u + 1], acc[4 * u + 2], acc[4 * u + 3]};
  bar();
  if (wave < 4) {
    f32x4 a = red[wave * 64 + lane];
#pragma unroll
    for (int w = 1; w < 8; ++w) a += red[(w * 4 + wave) * 64 + lane];
    *(u32x2*)(out + (size_t)(b * SQ + (lane & 31)) * 1024 + br * 512 + cb * 32 + 8 * wave + 4 * hi) = u32x2{cvtpk(a[0], a[1]), cvtpk(a[2], a[3])};
  }
}

extern "C" void kernel_launch(void* const* d_in, const int* in_sizes, int n_in, void* d_out, int out_size, void* d_ws, size_t ws_size, hipStream_t stream) {
  (void)in_sizes; (void)n_in; (void)out_size; (void)ws_size;
  (void)hipFuncSetAttribute((const void*)attn_kernel, hipFuncAttributeMaxDynamicSharedMemorySize, LDS1);
  (void)hipFuncSetAttribute((const void*)oproj_kernel, hipFuncAttributeMaxDynamicSharedMemorySize, LDS1);
  u32x4* ows = (u32x4*)d_ws;
  hipLaunchKernelGGL(attn_kernel, dim3(256), dim3(512), LDS1, stream, d_in[0], d_in[1], d_in[2], d_in[3], d_in[5], d_in[6], d_in[7], d_in[8], ows);
  hipLaunchKernelGGL(oproj_kernel, dim3(256), dim3(512), LDS1, stream, d_in[4], d_in[9], (const u32x4*)ows, (__hip_bfloat16*)d_out);
}
```

```cpp
#define DIAGK 1
#define DIAGM 0
#define DIAGW 0
#define POLS 0xC
#include <hip/hip_runtime.h>
#include <hip/hip_bf16.h>
#include <stdint.h>

#define DI __device__ __forceinline__
#define LAS __attribute__((address_space(3)))

typedef __attribute__((ext_vector_type(8))) __bf16 bf16x8;
typedef __attribute__((ext_vector_type(4))) __bf16 bf16x4;
typedef __attribute__((ext_vector_type(2))) __bf16 bf16x2;
typedef __attribute__((ext_vector_type(2))) float f32x2;
typedef __attribute__((ext_vector_type(4))) float f32x4;
typedef __attribute__((ext_vector_type(16))) float f32x16;
typedef __attribute__((ext_vector_type(4))) unsigned u32x4;
typedef __attribute__((ext_vector_type(2))) unsigned u32x2;
typedef __amdgpu_buffer_rsrc_t rsrc_t;

constexpr int NB = 8, SQ = 32, DM = 512, NH = 32, SK = 2048, DH = 128;
constexpr int NT = 16;
constexpr unsigned RING = 0, SCR = 131072;
constexpr unsigned LDS1 = 163840;
constexpr float SC2 = 0.08838834764831845f * 1.4426950408889634f;

#define STAMP(k)
#define NSTAMP 5
#define TAKE(k) st_[k] = __builtin_amdgcn_s_memrealtime()
#define VB_BMIN 0

DI unsigned rfl(unsigned x) { return __builtin_amdgcn_readfirstlane(x); }
DI rsrc_t mk_rsrc(const void* p, unsigned bytes) { return __builtin_amdgcn_make_buffer_rsrc((void*)p, 0, bytes, 0x00020000); }
DI unsigned cvtpk(float lo, float hi) { f32x2 v = {lo, hi}; bf16x2 b = __builtin_convertvector(v, bf16x2); return __builtin_bit_cast(unsigned, b); }
DI void bar() { asm volatile("s_waitcnt lgkmcnt(0)\n\ts_barrier" ::: "memory"); }

#define DMA_TILE(NTS) asm volatile( \
    "s_waitcnt lgkmcnt(0)\n\t" \
    "s_mov_b32 m0, %0\n\ts_nop 0\n\t" \
    "buffer_load_dwordx4 %5, %4, %2 offen" NTS " lds\n\t" \
    "buffer_load_dwordx4 %6, %4, %2 offen offset:1024" NTS " lds\n\t" \
    "buffer_load_dwordx4 %7, %4, %2 offen offset:2048" NTS " lds\n\t" \
    "buffer_load_dwordx4 %8, %4, %2 offen offset:3072" NTS " lds\n\t" \
    "s_mov_b32 m0, %1\n\ts_nop 0\n\t" \
    "buffer_load_dwordx4 %5, %4, %3 offen" NTS " lds\n\t" \
    "buffer_load_dwordx4 %6, %4, %3 offen offset:1024" NTS " lds\n\t" \
    "buffer_load_dwordx4 %7, %4, %3 offen offset:2048" NTS " lds\n\t" \
    "buffer_load_dwordx4 %8, %4, %3 offen offset:3072" NTS " lds" \
    :: "s"(lds), "s"(lds + 4096u), "s"(soff), "s"(soff + 4096u), "s"(srd), "v"(vo[0]), "v"(vo[1]), "v"(vo[2]), "v"(vo[3]) : "memory")
DI void dma_tile(int nt, unsigned lds, rsrc_t srd, unsigned soff, const unsigned (&vo)[4]) {
  if (nt) DMA_TILE(" nt"); else DMA_TILE("");
}
DI u32x4 bload(rsrc_t srd, unsigned voff, unsigned soff) {
  u32x4 r; asm volatile("buffer_load_dwordx4 %0, %1, %2, %3 offen" : "=v"(r) : "v"(voff), "s"(srd), "s"(soff) : "memory"); return r;
}

__global__ __launch_bounds__(512, 2) void attn_kernel(const void* __restrict__ xA, const void* __restrict__ kA, const void* __restrict__ vA, const void* __restrict__ wqA,
                                                      const void* __restrict__ xB, const void* __restrict__ kB, const void* __restrict__ vB, const void* __restrict__ wqB,
                                                      u32x4* __restrict__ ows) {
  extern __shared__ __attribute__((aligned(16))) char smem_[];
  LAS char* smem = (LAS char*)smem_;
  const unsigned lds0 = (unsigned)(uintptr_t)smem;
  const int lane = threadIdx.x & 63;
  const int wave = rfl(threadIdx.x >> 6), grp = wave >> 2, wv = wave & 3;
  unsigned long long st_[NSTAMP];
  TAKE(0);
  const int g8 = blockIdx.x & 7, jj = blockIdx.x >> 3, b = jj >> 2, head = 4 * g8 + (jj & 3);
  const size_t kvoff = (size_t)(grp ? b * 8 + g8 : b * 32 + head) * (SK * DH * 2);
  const rsrc_t srdK = mk_rsrc((const char*)(grp ? kA : kB) + kvoff, SK * DH * 2);
  const rsrc_t srdV = mk_rsrc((const char*)(grp ? vA : vB) + kvoff, SK * DH * 2);
  const rsrc_t srdW = mk_rsrc(grp ? wqA : wqB, DM * NH * DH * 2);
  const rsrc_t srdX = mk_rsrc((const char*)(grp ? xA : xB) + (size_t)b * (SQ * DM * 2), SQ * DM * 2);
  const int ntk = grp ? (POLS & 1) : ((POLS >> 2) & 1), ntv = grp ? ((POLS >> 1) & 1) : (((POLS >> 3) & 1) | (b < VB_BMIN));
  const unsigned kslot = RING + wave * 16384u, vslot = kslot + 8192u;
  unsigned vo[4];
#pragma unroll
  for (int i = 0; i < 4; ++i) vo[i] = (lane >> 4) * 256 + 16 * ((lane & 15) ^ (((lane >> 4) << 2) | i));

  bf16x8 qf[8];
  {
    const int c = lane & 15, g = lane >> 4;
    u32x4 xr[4][2], wr[4][8];
    const unsigned xv0 = (c + 16 * (wv >> 1)) * 1024 + g * 16, xv1 = (c + 16 * ((wv >> 1) ^ 1)) * 1024 + g * 16;
    const unsigned wvo = g * 65536 + 16 * (c ^ (2 * (wv & 1)));
#pragma unroll
    for (int ks = 0; ks < 4; ++ks) {
      xr[ks][0] = bload(srdX, xv0, wv * 256 + ks * 64);
      xr[ks][1] = bload(srdX, xv1, wv * 256 + ks * 64);
#pragma unroll
      for (int i = 0; i < 8; ++i) wr[ks][i] = bload(srdW, wvo, (wv * 128 + ks * 32 + i) * 8192 + head * 256);
    }
    dma_tile(ntk, lds0 + kslot, srdK, wv * 8192u, vo);
    dma_tile(ntv, lds0 + vslot, srdV, wv * 8192u, vo);
    f32x4 acc[8][2];
#pragma unroll
    for (int ks = 0; ks < 4; ++ks) {
#define QWAIT(N) asm volatile("s_waitcnt vmcnt(" #N ")" : "+v"(xr[ks][0]), "+v"(xr[ks][1]), "+v"(wr[ks][0]), "+v"(wr[ks][1]), "+v"(wr[ks][2]), "+v"(wr[ks][3]), \
                              "+v"(wr[ks][4]), "+v"(wr[ks][5]), "+v"(wr[ks][6]), "+v"(wr[ks][7]) :: "memory")
      __builtin_amdgcn_sched_barrier(0);
      if (ks == 0) QWAIT(46); else if (ks == 1) QWAIT(36); else if (ks == 2) QWAIT(26); else QWAIT(16);
#undef QWAIT
#pragma unroll
      for (int j = 0; j < 8; ++j) {
        u32x4 f;
#pragma unroll
        for (int m = 0; m < 4; ++m) f[m] = __builtin_amdgcn_perm(wr[ks][2 * m + 1][j >> 1], wr[ks][2 * m][j >> 1], (j & 1) ? 0x07060302u : 0x05040100u);
#pragma unroll
        for (int qb = 0; qb < 2; ++qb)
          acc[j][qb] = __builtin_amdgcn_mfma_f32_16x16x32_bf16(__builtin_bit_cast(bf16x8, f), __builtin_bit_cast(bf16x8, xr[ks][qb]), ks ? acc[j][qb] : f32x4{0.f, 0.f, 0.f, 0.f}, 0, 0, 0);
      }
    }
    TAKE(1);
    LAS f32x4* mine = (LAS f32x4*)(smem + SCR + wave * 4096) + lane;
#pragma unroll
    for (int r = 1; r < 4; ++r) {
      const int qb = r >> 1, rp = r & 1;
#pragma unroll
      for (int u = 0; u < 4; ++u) mine[u * 64] = f32x4{acc[2 * u][qb][2 * rp], acc[2 * u][qb][2 * rp + 1], acc[2 * u + 1][qb][2 * rp], acc[2 * u + 1][qb][2 * rp + 1]};
      bar();
      LAS f32x4* other = (LAS f32x4*)(smem + SCR + (wave ^ r) * 4096) + lane;
#pragma unroll
      for (int u = 0; u < 4; ++u) { const f32x4 v = other[u * 64]; acc[2 * u][0][0] += v[0]; acc[2 * u][0][1] += v[1]; acc[2 * u + 1][0][0] += v[2]; acc[2 * u + 1][0][1] += v[3]; }
      bar();
    }
    LAS char* qimg = smem + SCR + grp * 8192;
    const int q = c + 16 * (wv >> 1);
#pragma unroll
    for (int e = 0; e < 2; ++e) {
      const int ch = 4 * g + 2 * (wv & 1) + e;
      *(LAS u32x4*)(qimg + q * 256 + 16 * (ch ^ c)) = u32x4{cvtpk(acc[0][0][e], acc[1][0][e]), cvtpk(acc[2][0][e], acc[3][0][e]), cvtpk(acc[4][0][e], acc[5][0][e]), cvtpk(acc[6][0][e], acc[7][0][e])};
    }
    bar();
#pragma unroll
    for (int s = 0; s < 8; ++s) qf[s] = *(LAS bf16x8*)(qimg + (lane & 31) * 256 + 16 * ((2 * s + (lane >> 5)) ^ (lane & 15)));
  }

  TAKE(2);
  const int r32 = lane & 31, hi = lane >> 5;
  const unsigned kaddr = kslot + 256 * r32 + 16 * (hi ^ (((r32 & 3) << 2) | ((r32 >> 2) & 3)));
  const int blk = (lane >> 4) & 1, q4 = (lane & 15) >> 2, p4 = lane & 3;
  const unsigned vaddr = vslot + 256 * (4 * hi + q4) + 64 * q4 + 16 * ((2 * blk + (p4 & 1)) ^ hi) + 8 * (p4 >> 1);
  f32x16 o[4] = {};
  float m = -__builtin_inff(), l = 0.f;
#pragma unroll 1
  for (int t = 0; t < NT; ++t) {
    const bool more = t + 1 < NT;
    const unsigned soffn = (unsigned)((t + 1) * 4 + wv) * 8192u;
    asm volatile("s_waitcnt vmcnt(8)" ::: "memory");
    bf16x8 kf[8];
#pragma unroll
    for (int s = 0; s < 8; ++s) kf[s] = *(LAS bf16x8*)(smem + (kaddr ^ (32u * s)));
    if (more) dma_tile(ntk, lds0 + kslot, srdK, soffn, vo);
    f32x16 sc = {};
#pragma unroll
    for (int s = 0; s < 8; ++s) sc = __builtin_amdgcn_mfma_f32_32x32x16_bf16(kf[s], qf[s], sc, 0, 0, 0);
    float mx = sc[0];
#pragma unroll
    for (int i = 1; i < 16; ++i) mx = fmaxf(mx, sc[i]);
    { auto rr = __builtin_amdgcn_permlane32_swap(__float_as_uint(mx), __float_as_uint(mx), false, false); mx = fmaxf(__uint_as_float(rr[0]), __uint_as_float(rr[1])); }
    const float mn = fmaxf(m, mx), alpha = __builtin_amdgcn_exp2f((m - mn) * SC2), nm = -mn * SC2;
    m = mn;
    float ps = 0.f;
#pragma unroll
    for (int i = 0; i < 16; ++i) { sc[i] = __builtin_amdgcn_exp2f(fmaf(sc[i], SC2, nm)); ps += sc[i]; }
    l = fmaf(l, alpha, ps);
    bf16x8 pf[2];
#pragma unroll
    for (int s = 0; s < 2; ++s) pf[s] = __builtin_bit_cast(bf16x8, u32x4{cvtpk(sc[8 * s], sc[8 * s + 1]), cvtpk(sc[8 * s + 2], sc[8 * s + 3]), cvtpk(sc[8 * s + 4], sc[8 * s + 5]), cvtpk(sc[8 * s + 6], sc[8 * s + 7])});
#pragma unroll
    for (int c = 0; c < 4; ++c) o[c] *= alpha;
    if (more) asm volatile("s_waitcnt vmcnt(8)" ::: "memory"); else asm volatile("s_waitcnt vmcnt(0)" ::: "memory");
    bf16x8 vf[4][2];
#pragma unroll
    for (int c = 0; c < 4; ++c)
#pragma unroll
      for (int s = 0; s < 2; ++s) {
        const bf16x4 lo = __builtin_amdgcn_ds_read_tr16_b64_v4bf16((LAS bf16x4*)(smem + ((vaddr ^ (64u * c)) + 4096u * s)));
        const bf16x4 up = __builtin_amdgcn_ds_read_tr16_b64_v4bf16((LAS bf16x4*)(smem + ((vaddr ^ (64u * c) ^ 32u) + 4096u * s + 2048u)));
        vf[c][s] = __builtin_shufflevector(lo, up, 0, 1, 2, 3, 4, 5, 6, 7);
      }
    if (more) dma_tile(ntv, lds0 + vslot, srdV, soffn, vo);
#pragma unroll
    for (int s = 0; s < 2; ++s)
#pragma unroll
      for (int c = 0; c < 4; ++c) o[c] = __builtin_amdgcn_mfma_f32_32x32x16_bf16(vf[c][s], pf[s], o[c], 0, 0, 0);
  }

  TAKE(3);
  { auto rr = __builtin_amdgcn_permlane32_swap(__float_as_uint(l), __float_as_uint(l), false, false); l = __uint_as_float(rr[0]) + __uint_as_float(rr[1]); }
  LAS f32x2* ml = (LAS f32x2*)(smem + SCR + 16384);
  if (hi == 0) ml[wave * 32 + r32] = f32x2{m, l};
  bar();
  {
    f32x2 v[4]; float ms = -__builtin_inff(), L = 0.f;
#pragma unroll
    for (int w = 0; w < 4; ++w) { v[w] = ml[(grp * 4 + w) * 32 + r32]; ms = fmaxf(ms, v[w][0]); }
#pragma unroll
    for (int w = 0; w < 4; ++w) L = fmaf(__builtin_amdgcn_exp2f((v[w][0] - ms) * SC2), v[w][1], L);
    const float f = __builtin_amdgcn_exp2f((m - ms) * SC2) / L;
    LAS f32x4* mine = (LAS f32x4*)(smem + kslot) + lane;
#pragma unroll
    for (int c = 0; c < 4; ++c)
#pragma unroll
      for (int u = 0; u < 4; ++u) mine[(c * 4 + u) * 64] = f32x4{o[c][4 * u] * f, o[c][4 * u + 1] * f, o[c][4 * u + 2] * f, o[c][4 * u + 3] * f};
  }
  bar();
  {
    f32x4 a[4] = {};
#pragma unroll
    for (int w = 0; w < 4; ++w) {
      LAS f32x4* src = (LAS f32x4*)(smem + RING + (grp * 4 + w) * 16384 + wv * 4096) + lane;
#pragma unroll
      for (int u = 0; u < 4; ++u) a[u] += src[u * 64];
    }
    u32x4* dst = ows + ((size_t)(((grp ^ 1) * NB + b) * NH + head) * 8 + 2 * wv) * 64 + lane;
    dst[0]  = u32x4{cvtpk(a[0][0], a[0][1]), cvtpk(a[0][2], a[0][3]), cvtpk(a[1][0], a[1][1]), cvtpk(a[1][2], a[1][3])};
    dst[64] = u32x4{cvtpk(a[2][0], a[2][1]), cvtpk(a[2][2], a[2][3]), cvtpk(a[3][0], a[3][1]), cvtpk(a[3][2], a[3][3])};
  }
  TAKE(4);
  if (blockIdx.x == 0 && wave == DIAGW) { const unsigned long long until = st_[4] + DIAGM * (st_[DIAGK] - st_[0]); while (__builtin_amdgcn_s_memrealtime() < until) __builtin_amdgcn_s_sleep(2); }
}

__global__ __launch_bounds__(512, 2) void oproj_kernel(const void* __restrict__ woA, const void* __restrict__ woB, const u32x4* __restrict__ ows, __hip_bfloat16* __restrict__ out) {
  extern __shared__ __attribute__((aligned(16))) char smem_[];
  LAS char* smem = (LAS char*)smem_;
  const unsigned lds0 = (unsigned)(uintptr_t)smem;
  const int lane = threadIdx.x & 63, wave = rfl(threadIdx.x >> 6);
  const int g8 = blockIdx.x & 7, jj = blockIdx.x >> 3;
  const unsigned long long t0_ = __builtin_amdgcn_s_memrealtime();
  const int br = g8 >> 2, b = 4 * ((g8 >> 1) & 1) + (jj >> 3), cb = 8 * (g8 & 1) + (jj & 7);
  const rsrc_t srdW = mk_rsrc(br ? woB : woA, NH * DH * DM * 2);
  const rsrc_t srdO = mk_rsrc(ows + (size_t)(br * NB + b) * NH * 8 * 64, NH * 8 * 1024);
  const unsigned slot = wave * 16384u;
  const unsigned wvo = (lane >> 2) * 1024 + (lane & 3) * 16;
  const unsigned wso = wave * 512 * 1024 + cb * 64;
#define DMA_W(R) _Pragma("unroll") for (int i = 0; i < 16; ++i) \
    asm volatile("s_mov_b32 m0, %0\n\ts_nop 0\n\tbuffer_load_dwordx4 %1, %2, %3 offen lds" :: "s"(lds0 + slot + 1024u * i), "v"(wvo), "s"(srdW), "s"(wso + ((R) * 16 + i) * 16384u) : "memory")
  DMA_W(0);
  u32x4 of[32];
#pragma unroll
  for (int s = 0; s < 32; ++s) of[s] = bload(srdO, lane * 16, (wave * 32 + s) * 1024);
  const int hi = lane >> 5, blk = (lane >> 4) & 1, q4 = (lane & 15) >> 2, p4 = lane & 3;
  const unsigned waddr = slot + 64 * (8 * hi + q4) + 32 * blk + 8 * p4;
  bf16x8 wf[16];
  asm volatile("s_waitcnt vmcnt(32)" ::: "memory");
#define RD_W() _Pragma("unroll") for (int s = 0; s < 16; ++s) { \
    const bf16x4 lo = __builtin_amdgcn_ds_read_tr16_b64_v4bf16((LAS bf16x4*)(smem + waddr + 1024u * s)); \
    const bf16x4 up = __builtin_amdgcn_ds_read_tr16_b64_v4bf16((LAS bf16x4*)(smem + waddr + 1024u * s + 256u)); \
    wf[s] = __builtin_shufflevector(lo, up, 0, 1, 2, 3, 4, 5, 6, 7); }
  RD_W();
  asm volatile("s_waitcnt lgkmcnt(0)" ::: "memory");
  DMA_W(1);
  f32x16 acc = {};
  asm volatile("s_waitcnt vmcnt(32)" : "+v"(of[0]), "+v"(of[1]), "+v"(of[2]), "+v"(of[3]), "+v"(of[4]), "+v"(of[5]), "+v"(of[6]), "+v"(of[7]),
               "+v"(of[8]), "+v"(of[9]), "+v"(of[10]), "+v"(of[11]), "+v"(of[12]), "+v"(of[13]), "+v"(of[14]), "+v"(of[15]) :: "memory");
#pragma unroll
  for (int s = 0; s < 16; ++s) acc = __builtin_amdgcn_mfma_f32_32x32x16_bf16(wf[s], __builtin_bit_cast(bf16x8, of[s]), acc, 0, 0, 0);
  asm volatile("s_waitcnt vmcnt(0)" : "+v"(of[16]), "+v"(of[17]), "+v"(of[18]), "+v"(of[19]), "+v"(of[20]), "+v"(of[21]), "+v"(of[22]), "+v"(of[23]),
               "+v"(of[24]), "+v"(of[25]), "+v"(of[26]), "+v"(of[27]), "+v"(of[28]), "+v"(of[29]), "+v"(of[30]), "+v"(of[31]) :: "memory");
  RD_W();
#pragma unroll
  for (int s = 0; s < 16; ++s) acc = __builtin_amdgcn_mfma_f32_32x32x16_bf16(wf[s], __builtin_bit_cast(bf16x8, of[16 + s]), acc, 0, 0, 0);
  LAS f32x4* red = (LAS f32x4*)(smem + 131072);
#pragma unroll
  for (int u = 0; u < 4; ++u) red[(wave * 4 + u) * 64 + lane] = f32x4{acc[4 * u], acc[4 * u + 1], acc[4 * u + 2], acc[4 * u + 3]};
  bar();
  if (wave < 4) {
    f32x4 a = red[wave * 64 + lane];
#pragma unroll
    for (int w = 1; w < 8; ++w) a += red[(w * 4 + wave) * 64 + lane];
    *(u32x2*)(out + (size_t)(b * SQ + (lane & 31)) * 1024 + br * 512 + cb * 32 + 8 * wave + 4 * hi) = u32x2{cvtpk(a[0], a[1]), cvtpk(a[2], a[3])};
  }
}

extern "C" void kernel_launch(void* const* d_in, const int* in_sizes, int n_in, void* d_out, int out_size, void* d_ws, size_t ws_size, hipStream_t stream) {
  (void)in_sizes; (void)n_in; (void)out_size; (void)ws_size;
  (void)hipFuncSetAttribute((const void*)attn_kernel, hipFuncAttributeMaxDynamicSharedMemorySize, LDS1);
  (void)hipFuncSetAttribute((const void*)oproj_kernel, hipFuncAttributeMaxDynamicSharedMemorySize, LDS1);
  u32x4* ows = (u32x4*)d_ws;
  hipLaunchKernelGGL(attn_kernel, dim3(256), dim3(512), LDS1, stream, d_in[0], d_in[1], d_in[2], d_in[3], d_in[5], d_in[6], d_in[7], d_in[8], ows);
  hipLaunchKernelGGL(oproj_kernel, dim3(256), dim3(512), LDS1, stream, d_in[4], d_in[9], (const u32x4*)ows, (__hip_bfloat16*)d_out);
}
```

```cpp
#define DIAGX 0
#define DIAGK 4
#define DIAGG -1
#define DIAGM 0
#define STAMPS
#define POLS 0xC
#include <hip/hip_runtime.h>
#include <hip/hip_bf16.h>
#include <stdint.h>

#define DI __device__ __forceinline__
#define LAS __attribute__((address_space(3)))

typedef __attribute__((ext_vector_type(8))) __bf16 bf16x8;
typedef __attribute__((ext_vector_type(4))) __bf16 bf16x4;
typedef __attribute__((ext_vector_type(2))) __bf16 bf16x2;
typedef __attribute__((ext_vector_type(2))) float f32x2;
typedef __attribute__((ext_vector_type(4))) float f32x4;
typedef __attribute__((ext_vector_type(16))) float f32x16;
typedef __attribute__((ext_vector_type(4))) unsigned u32x4;
typedef __attribute__((ext_vector_type(2))) unsigned u32x2;
typedef __amdgpu_buffer_rsrc_t rsrc_t;

constexpr int NB = 8, SQ = 32, DM = 512, NH = 32, SK = 2048, DH = 128;
constexpr int NT = 16;
constexpr unsigned RING = 0, SCR = 131072;
constexpr unsigned LDS1 = 163840;
constexpr float SC2 = 0.08838834764831845f * 1.4426950408889634f;

#define NSTAMP 5
#define STAMP_OFF (64u << 20)
#define STAMP(k) do { if (lane == 0) ((unsigned long long*)((char*)ows + STAMP_OFF))[(blockIdx.x * 8 + wave) * NSTAMP + (k)] = st_[k]; } while (0)
#define TAKE(k) st_[k] = __builtin_amdgcn_s_memrealtime()
#define VB_BMIN 0

DI unsigned rfl(unsigned x) { return __builtin_amdgcn_readfirstlane(x); }
DI rsrc_t mk_rsrc(const void* p, unsigned bytes) { return __builtin_amdgcn_make_buffer_rsrc((void*)p, 0, bytes, 0x00020000); }
DI unsigned cvtpk(float lo, float hi) { f32x2 v = {lo, hi}; bf16x2 b = __builtin_convertvector(v, bf16x2); return __builtin_bit_cast(unsigned, b); }
DI void bar() { asm volatile("s_waitcnt lgkmcnt(0)\n\ts_barrier" ::: "memory"); }

#define DMA_TILE(NTS) asm volatile( \
    "s_waitcnt lgkmcnt(0)\n\t" \
    "s_mov_b32 m0, %0\n\ts_nop 0\n\t" \
    "buffer_load_dwordx4 %5, %4, %2 offen" NTS " lds\n\t" \
    "buffer_load_dwordx4 %6, %4, %2 offen offset:1024" NTS " lds\n\t" \
    "buffer_load_dwordx4 %7, %4, %2 offen offset:2048" NTS " lds\n\t" \
    "buffer_load_dwordx4 %8, %4, %2 offen offset:3072" NTS " lds\n\t" \
    "s_mov_b32 m0, %1\n\ts_nop 0\n\t" \
    "buffer_load_dwordx4 %5, %4, %3 offen" NTS " lds\n\t" \
    "buffer_load_dwordx4 %6, %4, %3 offen offset:1024" NTS " lds\n\t" \
    "buffer_load_dwordx4 %7, %4, %3 offen offset:2048" NTS " lds\n\t" \
    "buffer_load_dwordx4 %8, %4, %3 offen offset:3072" NTS " lds" \
    :: "s"(lds), "s"(lds + 4096u), "s"(soff), "s"(soff + 4096u), "s"(srd), "v"(vo[0]), "v"(vo[1]), "v"(vo[2]), "v"(vo[3]) : "memory")
DI void dma_tile(int nt, unsigned lds, rsrc_t srd, unsigned soff, const unsigned (&vo)[4]) {
  if (nt) DMA_TILE(" nt"); else DMA_TILE("");
}
DI u32x4 bload(rsrc_t srd, unsigned voff, unsigned soff) {
  u32x4 r; asm volatile("buffer_load_dwordx4 %0, %1, %2, %3 offen" : "=v"(r) : "v"(voff), "s"(srd), "s"(soff) : "memory"); return r;
}

__global__ __launch_bounds__(512, 2) void attn_kernel(const void* __restrict__ xA, const void* __restrict__ kA, const void* __restrict__ vA, const void* __restrict__ wqA,
                                                      const void* __restrict__ xB, const void* __restrict__ kB, const void* __restrict__ vB, const void* __restrict__ wqB,
                                                      u32x4* __restrict__ ows) {
  extern __shared__ __attribute__((aligned(16))) char smem_[];
  LAS char* smem = (LAS char*)smem_;
  const unsigned lds0 = (unsigned)(uintptr_t)smem;
  const int lane = threadIdx.x & 63;
  const int wave = rfl(threadIdx.x >> 6), grp = wave >> 2, wv = wave & 3;
  unsigned long long st_[NSTAMP];
  TAKE(0);
  const int g8 = blockIdx.x & 7, jj = blockIdx.x >> 3, b = jj >> 2, head = 4 * g8 + (jj & 3);
  const size_t kvoff = (size_t)(grp ? b * 8 + g8 : b * 32 + head) * (SK * DH * 2);
  const rsrc_t srdK = mk_rsrc((const char*)(grp ? kA : kB) + kvoff, SK * DH * 2);
  const rsrc_t srdV = mk_rsrc((const char*)(grp ? vA : vB) + kvoff, SK * DH * 2);
  const rsrc_t srdW = mk_rsrc(grp ? wqA : wqB, DM * NH * DH * 2);
  const rsrc_t srdX = mk_rsrc((const char*)(grp ? xA : xB) + (size_t)b * (SQ * DM * 2), SQ * DM * 2);
  const int ntk = grp ? (POLS & 1) : ((POLS >> 2) & 1), ntv = grp ? ((POLS >> 1) & 1) : (((POLS >> 3) & 1) | (b < VB_BMIN));
  const unsigned kslot = RING + wave * 16384u, vslot = kslot + 8192u;
  unsigned vo[4];
#pragma unroll
  for (int i = 0; i < 4; ++i) vo[i] = (lane >> 4) * 256 + 16 * ((lane & 15) ^ (((lane >> 4) << 2) | i));

  bf16x8 qf[8];
  {
    const int c = lane & 15, g = lane >> 4;
    u32x4 xr[4][2], wr[4][8];
    const unsigned xv0 = (c + 16 * (wv >> 1)) * 1024 + g * 16, xv1 = (c + 16 * ((wv >> 1) ^ 1)) * 1024 + g * 16;
    const unsigned wvo = g * 65536 + 16 * (c ^ (2 * (wv & 1)));
#pragma unroll
    for (int ks = 0; ks < 4; ++ks) {
      xr[ks][0] = bload(srdX, xv0, wv * 256 + ks * 64);
      xr[ks][1] = bload(srdX, xv1, wv * 256 + ks * 64);
#pragma unroll
      for (int i = 0; i < 8; ++i) wr[ks][i] = bload(srdW, wvo, (wv * 128 + ks * 32 + i) * 8192 + head * 256);
    }
    dma_tile(ntk, lds0 + kslot, srdK, wv * 8192u, vo);
    dma_tile(ntv, lds0 + vslot, srdV, wv * 8192u, vo);
    f32x4 acc[8][2];
#pragma unroll
    for (int ks = 0; ks < 4; ++ks) {
#define QWAIT(N) asm volatile("s_waitcnt vmcnt(" #N ")" : "+v"(xr[ks][0]), "+v"(xr[ks][1]), "+v"(wr[ks][0]), "+v"(wr[ks][1]), "+v"(wr[ks][2]), "+v"(wr[ks][3]), \
                              "+v"(wr[ks][4]), "+v"(wr[ks][5]), "+v"(wr[ks][6]), "+v"(wr[ks][7]) :: "memory")
      __builtin_amdgcn_sched_barrier(0);
      if (ks == 0) QWAIT(46); else if (ks == 1) QWAIT(36); else if (ks == 2) QWAIT(26); else QWAIT(16);
#undef QWAIT
#pragma unroll
      for (int j = 0; j < 8; ++j) {
        u32x4 f;
#pragma unroll
        for (int m = 0; m < 4; ++m) f[m] = __builtin_amdgcn_perm(wr[ks][2 * m + 1][j >> 1], wr[ks][2 * m][j >> 1], (j & 1) ? 0x07060302u : 0x05040100u);
#pragma unroll
        for (int qb = 0; qb < 2; ++qb)
          acc[j][qb] = __builtin_amdgcn_mfma_f32_16x16x32_bf16(__builtin_bit_cast(bf16x8, f), __builtin_bit_cast(bf16x8, xr[ks][qb]), ks ? acc[j][qb] : f32x4{0.f, 0.f, 0.f, 0.f}, 0, 0, 0);
      }
    }
    TAKE(1);
    LAS f32x4* mine = (LAS f32x4*)(smem + SCR + wave * 4096) + lane;
#pragma unroll
    for (int r = 1; r < 4; ++r) {
      const int qb = r >> 1, rp = r & 1;
#pragma unroll
      for (int u = 0; u < 4; ++u) mine[u * 64] = f32x4{acc[2 * u][qb][2 * rp], acc[2 * u][qb][2 * rp + 1], acc[2 * u + 1][qb][2 * rp], acc[2 * u + 1][qb][2 * rp + 1]};
      bar();
      LAS f32x4* other = (LAS f32x4*)(smem + SCR + (wave ^ r) * 4096) + lane;
#pragma unroll
      for (int u = 0; u < 4; ++u) { const f32x4 v = other[u * 64]; acc[2 * u][0][0] += v[0]; acc[2 * u][0][1] += v[1]; acc[2 * u + 1][0][0] += v[2]; acc[2 * u + 1][0][1] += v[3]; }
      bar();
    }
    LAS char* qimg = smem + SCR + grp * 8192;
    const int q = c + 16 * (wv >> 1);
#pragma unroll
    for (int e = 0; e < 2; ++e) {
      const int ch = 4 * g + 2 * (wv & 1) + e;
      *(LAS u32x4*)(qimg + q * 256 + 16 * (ch ^ c)) = u32x4{cvtpk(acc[0][0][e], acc[1][0][e]), cvtpk(acc[2][0][e], acc[3][0][e]), cvtpk(acc[4][0][e], acc[5][0][e]), cvtpk(acc[6][0][e], acc[7][0][e])};
    }
    bar();
#pragma unroll
    for (int s = 0; s < 8; ++s) qf[s] = *(LAS bf16x8*)(qimg + (lane & 31) * 256 + 16 * ((2 * s + (lane >> 5)) ^ (lane & 15)));
  }

  TAKE(2);
  const int r32 = lane & 31, hi = lane >> 5;
  const unsigned kaddr = kslot + 256 * r32 + 16 * (hi ^ (((r32 & 3) << 2) | ((r32 >> 2) & 3)));
  const int blk = (lane >> 4) & 1, q4 = (lane & 15) >> 2, p4 = lane & 3;
  const unsigned vaddr = vslot + 256 * (4 * hi + q4) + 64 * q4 + 16 * ((2 * blk + (p4 & 1)) ^ hi) + 8 * (p4 >> 1);
  f32x16 o[4] = {};
  float m = -__builtin_inff(), l = 0.f;
#pragma unroll 1
  for (int t = 0; t < NT; ++t) {
    const bool more = t + 1 < NT;
    const unsigned soffn = (unsigned)((t + 1) * 4 + wv) * 8192u;
    asm volatile("s_waitcnt vmcnt(8)" ::: "memory");
    bf16x8 kf[8];
#pragma unroll
    for (int s = 0; s < 8; ++s) kf[s] = *(LAS bf16x8*)(smem + (kaddr ^ (32u * s)));
    if (more) dma_tile(ntk, lds0 + kslot, srdK, soffn, vo);
    f32x16 sc = {};
#pragma unroll
    for (int s = 0; s < 8; ++s) sc = __builtin_amdgcn_mfma_f32_32x32x16_bf16(kf[s], qf[s], sc, 0, 0, 0);
    float mx = sc[0];
#pragma unroll
    for (int i = 1; i < 16; ++i) mx = fmaxf(mx, sc[i]);
    { auto rr = __builtin_amdgcn_permlane32_swap(__float_as_uint(mx), __float_as_uint(mx), false, false); mx = fmaxf(__uint_as_float(rr[0]), __uint_as_float(rr[1])); }
    const float mn = fmaxf(m, mx), alpha = __builtin_amdgcn_exp2f((m - mn) * SC2), nm = -mn * SC2;
    m = mn;
    float ps = 0.f;
#pragma unroll
    for (int i = 0; i < 16; ++i) { sc[i] = __builtin_amdgcn_exp2f(fmaf(sc[i], SC2, nm)); ps += sc[i]; }
    l = fmaf(l, alpha, ps);
    bf16x8 pf[2];
#pragma unroll
    for (int s = 0; s < 2; ++s) pf[s] = __builtin_bit_cast(bf16x8, u32x4{cvtpk(sc[8 * s], sc[8 * s + 1]), cvtpk(sc[8 * s + 2], sc[8 * s + 3]), cvtpk(sc[8 * s + 4], sc[8 * s + 5]), cvtpk(sc[8 * s + 6], sc[8 * s + 7])});
#pragma unroll
    for (int c = 0; c < 4; ++c) o[c] *= alpha;
    if (more) asm volatile("s_waitcnt vmcnt(8)" ::: "memory"); else asm volatile("s_waitcnt vmcnt(0)" ::: "memory");
    bf16x8 vf[4][2];
#pragma unroll
    for (int c = 0; c < 4; ++c)
#pragma unroll
      for (int s = 0; s < 2; ++s) {
        const bf16x4 lo = __builtin_amdgcn_ds_read_tr16_b64_v4bf16((LAS bf16x4*)(smem + ((vaddr ^ (64u * c)) + 4096u * s)));
        const bf16x4 up = __builtin_amdgcn_ds_read_tr16_b64_v4bf16((LAS bf16x4*)(smem + ((vaddr ^ (64u * c) ^ 32u) + 4096u * s + 2048u)));
        vf[c][s] = __builtin_shufflevector(lo, up, 0, 1, 2, 3, 4, 5, 6, 7);
      }
    if (more) dma_tile(ntv, lds0 + vslot, srdV, soffn, vo);
#pragma unroll
    for (int s = 0; s < 2; ++s)
#pragma unroll
      for (int c = 0; c < 4; ++c) o[c] = __builtin_amdgcn_mfma_f32_32x32x16_bf16(vf[c][s], pf[s], o[c], 0, 0, 0);
  }

  TAKE(3);
  { auto rr = __builtin_amdgcn_permlane32_swap(__float_as_uint(l), __float_as_uint(l), false, false); l = __uint_as_float(rr[0]) + __uint_as_float(rr[1]); }
  LAS f32x2* ml = (LAS f32x2*)(smem + SCR + 16384);
  if (hi == 0) ml[wave * 32 + r32] = f32x2{m, l};
  bar();
  {
    f32x2 v[4]; float ms = -__builtin_inff(), L = 0.f;
#pragma unroll
    for (int w = 0; w < 4; ++w) { v[w] = ml[(grp * 4 + w) * 32 + r32]; ms = fmaxf(ms, v[w][0]); }
#pragma unroll
    for (int w = 0; w < 4; ++w) L = fmaf(__builtin_amdgcn_exp2f((v[w][0] - ms) * SC2), v[w][1], L);
    const float f = __builtin_amdgcn_exp2f((m - ms) * SC2) / L;
    LAS f32x4* mine = (LAS f32x4*)(smem + kslot) + lane;
#pragma unroll
    for (int c = 0; c < 4; ++c)
#pragma unroll
      for (int u = 0; u < 4; ++u) mine[(c * 4 + u) * 64] = f32x4{o[c][4 * u] * f, o[c][4 * u + 1] * f, o[c][4 * u + 2] * f, o[c][4 * u + 3] * f};
  }
  bar();
  {
    f32x4 a[4] = {};
#pragma unroll
    for (int w = 0; w < 4; ++w) {
      LAS f32x4* src = (LAS f32x4*)(smem + RING + (grp * 4 + w) * 16384 + wv * 4096) + lane;
#pragma unroll
      for (int u = 0; u < 4; ++u) a[u] += src[u * 64];
    }
    u32x4* dst = ows + ((size_t)(((grp ^ 1) * NB + b) * NH + head) * 8 + 2 * wv) * 64 + lane;
    dst[0]  = u32x4{cvtpk(a[0][0], a[0][1]), cvtpk(a[0][2], a[0][3]), cvtpk(a[1][0], a[1][1]), cvtpk(a[1][2], a[1][3])};
    dst[64] = u32x4{cvtpk(a[2][0], a[2][1]), cvtpk(a[2][2], a[2][3]), cvtpk(a[3][0], a[3][1]), cvtpk(a[3][2], a[3][3])};
  }
  TAKE(4);
  STAMP(0); STAMP(1); STAMP(2); STAMP(3); STAMP(4);
}

__global__ __launch_bounds__(512, 2) void oproj_kernel(const void* __restrict__ woA, const void* __restrict__ woB, const u32x4* __restrict__ ows, __hip_bfloat16* __restrict__ out) {
  extern __shared__ __attribute__((aligned(16))) char smem_[];
  LAS char* smem = (LAS char*)smem_;
  const unsigned lds0 = (unsigned)(uintptr_t)smem;
  const int lane = threadIdx.x & 63, wave = rfl(threadIdx.x >> 6);
  const int g8 = blockIdx.x & 7, jj = blockIdx.x >> 3;
  const unsigned long long t0_ = __builtin_amdgcn_s_memrealtime();
  const int br = g8 >> 2, b = 4 * ((g8 >> 1) & 1) + (jj >> 3), cb = 8 * (g8 & 1) + (jj & 7);
  const rsrc_t srdW = mk_rsrc(br ? woB : woA, NH * DH * DM * 2);
  const rsrc_t srdO = mk_rsrc(ows + (size_t)(br * NB + b) * NH * 8 * 64, NH * 8 * 1024);
  const unsigned slot = wave * 16384u;
  const unsigned wvo = (lane >> 2) * 1024 + (lane & 3) * 16;
  const unsigned wso = wave * 512 * 1024 + cb * 64;
#define DMA_W(R) _Pragma("unroll") for (int i = 0; i < 16; ++i) \
    asm volatile("s_mov_b32 m0, %0\n\ts_nop 0\n\tbuffer_load_dwordx4 %1, %2, %3 offen lds" :: "s"(lds0 + slot + 1024u * i), "v"(wvo), "s"(srdW), "s"(wso + ((R) * 16 + i) * 16384u) : "memory")
  DMA_W(0);
  u32x4 of[32];
#pragma unroll
  for (int s = 0; s < 32; ++s) of[s] = bload(srdO, lane * 16, (wave * 32 + s) * 1024);
  const int hi = lane >> 5, blk = (lane >> 4) & 1, q4 = (lane & 15) >> 2, p4 = lane & 3;
  const unsigned waddr = slot + 64 * (8 * hi + q4) + 32 * blk + 8 * p4;
  bf16x8 wf[16];
  asm volatile("s_waitcnt vmcnt(32)" ::: "memory");
#define RD_W() _Pragma("unroll") for (int s = 0; s < 16; ++s) { \
    const bf16x4 lo = __builtin_amdgcn_ds_read_tr16_b64_v4bf16((LAS bf16x4*)(smem + waddr + 1024u * s)); \
    const bf16x4 up = __builtin_amdgcn_ds_read_tr16_b64_v4bf16((LAS bf16x4*)(smem + waddr + 1024u * s + 256u)); \
    wf[s] = __builtin_shufflevector(lo, up, 0, 1, 2, 3, 4, 5, 6, 7); }
  RD_W();
  asm volatile("s_waitcnt lgkmcnt(0)" ::: "memory");
  DMA_W(1);
  f32x16 acc = {};
  asm volatile("s_waitcnt vmcnt(32)" : "+v"(of[0]), "+v"(of[1]), "+v"(of[2]), "+v"(of[3]), "+v"(of[4]), "+v"(of[5]), "+v"(of[6]), "+v"(of[7]),
               "+v"(of[8]), "+v"(of[9]), "+v"(of[10]), "+v"(of[11]), "+v"(of[12]), "+v"(of[13]), "+v"(of[14]), "+v"(of[15]) :: "memory");
#pragma unroll
  for (int s = 0; s < 16; ++s) acc = __builtin_amdgcn_mfma_f32_32x32x16_bf16(wf[s], __builtin_bit_cast(bf16x8, of[s]), acc, 0, 0, 0);
  asm volatile("s_waitcnt vmcnt(0)" : "+v"(of[16]), "+v"(of[17]), "+v"(of[18]), "+v"(of[19]), "+v"(of[20]), "+v"(of[21]), "+v"(of[22]), "+v"(of[23]),
               "+v"(of[24]), "+v"(of[25]), "+v"(of[26]), "+v"(of[27]), "+v"(of[28]), "+v"(of[29]), "+v"(of[30]), "+v"(of[31]) :: "memory");
  RD_W();
#pragma unroll
  for (int s = 0; s < 16; ++s) acc = __builtin_amdgcn_mfma_f32_32x32x16_bf16(wf[s], __builtin_bit_cast(bf16x8, of[16 + s]), acc, 0, 0, 0);
  LAS f32x4* red = (LAS f32x4*)(smem + 131072);
#pragma unroll
  for (int u = 0; u < 4; ++u) red[(wave * 4 + u) * 64 + lane] = f32x4{acc[4 * u], acc[4 * u + 1], acc[4 * u + 2], acc[4 * u + 3]};
  bar();
  if (wave < 4) {
    f32x4 a = red[wave * 64 + lane];
#pragma unroll
    for (int w = 1; w < 8; ++w) a += red[(w * 4 + wave) * 64 + lane];
    *(u32x2*)(out + (size_t)(b * SQ + (lane & 31)) * 1024 + br * 512 + cb * 32 + 8 * wave + 4 * hi) = u32x2{cvtpk(a[0], a[1]), cvtpk(a[2], a[3])};
  }
  if (blockIdx.x == 0 && wave == 0) {
    const unsigned long long* st = (const unsigned long long*)((const char*)ows + STAMP_OFF);
    unsigned long long mn = ~0ull, mx = 0, mk = 0;
    for (int w = lane; w < 2048; w += 64) { mn = st[w * NSTAMP] < mn ? st[w * NSTAMP] : mn; mx = st[w * NSTAMP + 4] > mx ? st[w * NSTAMP + 4] : mx;
      if (DIAGG < 0 || ((w >> 2) & 1) == DIAGG) mk = st[w * NSTAMP + DIAGK] > mk ? st[w * NSTAMP + DIAGK] : mk; }
    for (int d = 1; d < 64; d <<= 1) { unsigned long long a = __shfl_xor(mn, d), c = __shfl_xor(mx, d), e = __shfl_xor(mk, d); mn = a < mn ? a : mn; mx = c > mx ? c : mx; mk = e > mk ? e : mk; }
    const unsigned long long X = DIAGX == 0 ? mk - mn : t0_ - mx;
    const unsigned long long t1_ = __builtin_amdgcn_s_memrealtime(), until = t1_ + DIAGM * (X > 100000 ? 100000 : X); while (__builtin_amdgcn_s_memrealtime() < until) __builtin_amdgcn_s_sleep(2);
  }
}

extern "C" void kernel_launch(void* const* d_in, const int* in_sizes, int n_in, void* d_out, int out_size, void* d_ws, size_t ws_size, hipStream_t stream) {
  (void)in_sizes; (void)n_in; (void)out_size; (void)ws_size;
  (void)hipFuncSetAttribute((const void*)attn_kernel, hipFuncAttributeMaxDynamicSharedMemorySize, LDS1);
  (void)hipFuncSetAttribute((const void*)oproj_kernel, hipFuncAttributeMaxDynamicSharedMemorySize, LDS1);
  u32x4* ows = (u32x4*)d_ws;
  hipLaunchKernelGGL(attn_kernel, dim3(256), dim3(512), LDS1, stream, d_in[0], d_in[1], d_in[2], d_in[3], d_in[5], d_in[6], d_in[7], d_in[8], ows);
  hipLaunchKernelGGL(oproj_kernel, dim3(256), dim3(512), LDS1, stream, d_in[4], d_in[9], (const u32x4*)ows, (__hip_bfloat16*)d_out);
}
```

```cpp
#include <hip/hip_runtime.h>
#include <hip/hip_bf16.h>
#include <stdint.h>

#define DI __device__ __forceinline__
#define LAS __attribute__((address_space(3)))
#define GAS __attribute__((address_space(1)))

typedef __attribute__((ext_vector_type(8))) __bf16 bf16x8;
typedef __attribute__((ext_vector_type(4))) __bf16 bf16x4;
typedef __attribute__((ext_vector_type(2))) __bf16 bf16x2;
typedef __attribute__((ext_vector_type(2))) float f32x2;
typedef __attribute__((ext_vector_type(4))) float f32x4;
typedef __attribute__((ext_vector_type(16))) float f32x16;
typedef __attribute__((ext_vector_type(4))) unsigned u32x4;
typedef __attribute__((ext_vector_type(2))) unsigned u32x2;
typedef __amdgpu_buffer_rsrc_t rsrc_t;

constexpr int NB = 8, SQ = 32, DM = 512, NH = 32, SK = 2048, DH = 128;
constexpr int NT = 32;
constexpr unsigned SCR = 131072;
constexpr unsigned LDS1 = 163840;
constexpr float SC2 = 0.08838834764831845f * 1.4426950408889634f;
constexpr unsigned WS_GEN = 0, WS_FLAG = 4096, WS_O = 65536, WS_BOX = WS_O + (4u << 20);
constexpr unsigned SPIN_CAP = 1u << 17;

#define A_OLD 1
#define NS_A 2
#define NS_B 2
#define NT_A 0
#define NT_B 1
#define OMAP 0
#define QBUF 3
#define TAKE(k)

DI unsigned rfl(unsigned x) { return __builtin_amdgcn_readfirstlane(x); }
DI rsrc_t mk_rsrc(const void* p, unsigned bytes) { return __builtin_amdgcn_make_buffer_rsrc((void*)p, 0, bytes, 0x00020000); }
DI unsigned cvtpk(float lo, float hi) { f32x2 v = {lo, hi}; bf16x2 b = __builtin_convertvector(v, bf16x2); return __builtin_bit_cast(unsigned, b); }
DI void bar() { asm volatile("s_waitcnt lgkmcnt(0)\n\ts_barrier" ::: "memory"); }
template <int N> DI void vmwait() { asm volatile("s_waitcnt vmcnt(%0)" :: "n"(N) : "memory"); }
#define SBAR() __builtin_amdgcn_sched_barrier(0)

#define DMA_TILE(NTS) asm volatile( \
    "s_waitcnt lgkmcnt(0)\n\t" \
    "s_mov_b32 m0, %0\n\ts_nop 0\n\t" \
    "buffer_load_dwordx4 %5, %4, %2 offen" NTS " lds\n\t" \
    "buffer_load_dwordx4 %6, %4, %2 offen offset:1024" NTS " lds\n\t" \
    "buffer_load_dwordx4 %7, %4, %2 offen offset:2048" NTS " lds\n\t" \
    "buffer_load_dwordx4 %8, %4, %2 offen offset:3072" NTS " lds\n\t" \
    "s_mov_b32 m0, %1\n\ts_nop 0\n\t" \
    "buffer_load_dwordx4 %5, %4, %3 offen" NTS " lds\n\t" \
    "buffer_load_dwordx4 %6, %4, %3 offen offset:1024" NTS " lds\n\t" \
    "buffer_load_dwordx4 %7, %4, %3 offen offset:2048" NTS " lds\n\t" \
    "buffer_load_dwordx4 %8, %4, %3 offen offset:3072" NTS " lds" \
    :: "s"(lds), "s"(lds + 4096u), "s"(soff), "s"(soff + 4096u), "s"(srd), "v"(vo[0]), "v"(vo[1]), "v"(vo[2]), "v"(vo[3]) : "memory")
template <int NTP> DI void dma_tile(unsigned lds, rsrc_t srd, unsigned soff, const unsigned (&vo)[4]) {
  if constexpr (NTP) DMA_TILE(" nt"); else DMA_TILE("");
}
DI u32x4 bload(rsrc_t srd, unsigned voff, unsigned soff) {
  u32x4 r; asm volatile("buffer_load_dwordx4 %0, %1, %2, %3 offen" : "=v"(r) : "v"(voff), "s"(srd), "s"(soff) : "memory"); return r;
}
DI u32x4 bload_sc1(rsrc_t srd, unsigned voff, unsigned soff) {
  u32x4 r; asm volatile("buffer_load_dwordx4 %0, %1, %2, %3 offen sc1" : "=v"(r) : "v"(voff), "s"(srd), "s"(soff) : "memory"); return r;
}

struct Ctx { LAS char* smem; unsigned lds0, ring; rsrc_t srdK, srdV; unsigned vo[4]; int lane, kh; };

template <int NS, int NTP> DI void qproj(const Ctx& c, rsrc_t srdX, rsrc_t srdW, int head, int rot, f32x4 (&acc)[8][2]) {
  const int cc = c.lane & 15, g = c.lane >> 4;
  u32x4 xr[8][2], wr[8][8];
  const unsigned xv0 = (cc + 16 * c.kh) * 1024 + g * 16, xv1 = (cc + 16 * (c.kh ^ 1)) * 1024 + g * 16;
  const unsigned wvo = g * 65536 + 16 * cc;
#define QISSUE(ks) do { const unsigned kk = (unsigned)(c.kh * 8 + (((ks) + rot) & 7)); \
    xr[ks][0] = bload(srdX, xv0, kk * 64); xr[ks][1] = bload(srdX, xv1, kk * 64); \
    _Pragma("unroll") for (int i = 0; i < 8; ++i) wr[ks][i] = bload(srdW, wvo, (kk * 32 + i) * 8192 + head * 256); } while (0)
#define KVISSUE(j) do { if constexpr ((j) < NS) dma_tile<NTP>(c.lds0 + c.ring + (j) * 8192u, ((j) & 1) ? c.srdV : c.srdK, (unsigned)(((j) >> 1) * 2 + c.kh) * 8192u, c.vo); } while (0)
#pragma unroll
  for (int ks = 0; ks < QBUF; ++ks) QISSUE(ks);
#pragma unroll
  for (int ks = 0; ks < 8; ++ks) {
    SBAR();
    const int nq = (ks + QBUF - 1 < 7 ? ks + QBUF - 1 : 7) - ks;
    const int lo = ks - QBUF + 1 > 0 ? ks - QBUF + 1 : 0;
    const int hi = ks < NS ? ks : NS;
    const int nd = hi > lo ? hi - lo : 0;
    const int n = nq * 10 + nd * 8;
#define QW(N) asm volatile("s_waitcnt vmcnt(%10)" : "+v"(xr[ks][0]), "+v"(xr[ks][1]), "+v"(wr[ks][0]), "+v"(wr[ks][1]), "+v"(wr[ks][2]), "+v"(wr[ks][3]), \
                           "+v"(wr[ks][4]), "+v"(wr[ks][5]), "+v"(wr[ks][6]), "+v"(wr[ks][7]) : "n"(N) : "memory")
    QW(n);
    if (ks == 0) KVISSUE(0); if (ks == 1) KVISSUE(1); if (ks == 2) KVISSUE(2); if (ks == 3) KVISSUE(3);
    if (ks + QBUF < 8) QISSUE((ks + QBUF) & 7);
    SBAR();
#pragma unroll
    for (int j = 0; j < 8; ++j) {
      u32x4 f;
#pragma unroll
      for (int m = 0; m < 4; ++m) f[m] = __builtin_amdgcn_perm(wr[ks][2 * m + 1][j >> 1], wr[ks][2 * m][j >> 1], (j & 1) ? 0x07060302u : 0x05040100u);
#pragma unroll
      for (int qb = 0; qb < 2; ++qb)
        acc[j][qb] = __builtin_amdgcn_mfma_f32_16x16x32_bf16(__builtin_bit_cast(bf16x8, f), __builtin_bit_cast(bf16x8, xr[ks][qb]), ks ? acc[j][qb] : f32x4{0.f, 0.f, 0.f, 0.f}, 0, 0, 0);
    }
  }
#undef QW
#undef QISSUE
#undef KVISSUE
}

template <int NS, int NTP> DI void attn_loop(const Ctx& c, const bf16x8 (&qf)[8], f32x16 (&o)[4], float& m, float& l) {
  const int lane = c.lane, r32 = lane & 31, hi = lane >> 5;
  const unsigned kaddr = c.ring + 256 * r32 + 16 * (hi ^ (((r32 & 3) << 2) | ((r32 >> 2) & 3)));
  const int blk = (lane >> 4) & 1, q4 = (lane & 15) >> 2, p4 = lane & 3;
  const unsigned vaddr = c.ring + 8192 + 256 * (4 * hi + q4) + 64 * q4 + 16 * ((2 * blk + (p4 & 1)) ^ hi) + 8 * (p4 >> 1);
  const unsigned vx = c.kh * 128u;
  constexpr int LA = NS / 2;
#pragma unroll 1
  for (int t0 = 0; t0 < NT; t0 += LA) {
#pragma unroll
    for (int u = 0; u < LA; ++u) {
      const int t = t0 + u;
      const unsigned so = u * 16384u;
      const bool more = t + LA < NT;
      const unsigned soffn = (unsigned)((t + LA) * 2 + c.kh) * 8192u;
      if (more || u == 0) vmwait<(NS - 1) * 8>(); else vmwait<(NS - 3) * 8 < 0 ? 0 : (NS - 3) * 8>();
      bf16x8 kf[8];
#pragma unroll
      for (int s = 0; s < 8; ++s) kf[s] = *(LAS bf16x8*)(c.smem + (kaddr ^ (32u * s)) + so);
      if (more) dma_tile<NTP>(c.lds0 + c.ring + so, c.srdK, soffn, c.vo);
      f32x16 sc = {};
#pragma unroll
      for (int s = 0; s < 8; ++s) sc = __builtin_amdgcn_mfma_f32_32x32x16_bf16(kf[s], qf[s], sc, 0, 0, 0);
      float mx = sc[0];
#pragma unroll
      for (int i = 1; i < 16; ++i) mx = fmaxf(mx, sc[i]);
      { auto rr = __builtin_amdgcn_permlane32_swap(__float_as_uint(mx), __float_as_uint(mx), false, false); mx = fmaxf(__uint_as_float(rr[0]), __uint_as_float(rr[1])); }
      const float mn = fmaxf(m, mx), alpha = __builtin_amdgcn_exp2f((m - mn) * SC2), nm = -mn * SC2;
      m = mn;
      float ps = 0.f;
#pragma unroll
      for (int i = 0; i < 16; ++i) { sc[i] = __builtin_amdgcn_exp2f(fmaf(sc[i], SC2, nm)); ps += sc[i]; }
      l = fmaf(l, alpha, ps);
      bf16x8 pf[2];
#pragma unroll
      for (int s = 0; s < 2; ++s) pf[s] = __builtin_bit_cast(bf16x8, u32x4{cvtpk(sc[8 * s], sc[8 * s + 1]), cvtpk(sc[8 * s + 2], sc[8 * s + 3]), cvtpk(sc[8 * s + 4], sc[8 * s + 5]), cvtpk(sc[8 * s + 6], sc[8 * s + 7])});
#pragma unroll
      for (int cb = 0; cb < 4; ++cb) o[cb] *= alpha;
      if (more) vmwait<(NS - 1) * 8>(); else if (u == 0) vmwait<(NS - 2) * 8>(); else vmwait<0>();
      bf16x8 vf[4][2];
#pragma unroll
      for (int cb = 0; cb < 4; ++cb)
#pragma unroll
        for (int s = 0; s < 2; ++s) {
          const bf16x4 lo = __builtin_amdgcn_ds_read_tr16_b64_v4bf16((LAS bf16x4*)(c.smem + ((vaddr ^ vx ^ (64u * cb)) + 4096u * s + so)));
          const bf16x4 up = __builtin_amdgcn_ds_read_tr16_b64_v4bf16((LAS bf16x4*)(c.smem + ((vaddr ^ vx ^ (64u * cb) ^ 32u) + 4096u * s + 2048u + so)));
          vf[cb][s] = __builtin_shufflevector(lo, up, 0, 1, 2, 3, 4, 5, 6, 7);
        }
      if (more) dma_tile<NTP>(c.lds0 + c.ring + 8192u + so, c.srdV, soffn, c.vo);
#pragma unroll
      for (int s = 0; s < 2; ++s)
#pragma unroll
        for (int cb = 0; cb < 4; ++cb) o[cb] = __builtin_amdgcn_mfma_f32_32x32x16_bf16(vf[cb][s], pf[s], o[cb], 0, 0, 0);
    }
  }
}

__global__ __launch_bounds__(256, 2) void fused_kernel(const void* __restrict__ xA, const void* __restrict__ kA, const void* __restrict__ vA, const void* __restrict__ wqA, const void* __restrict__ woA,
                                                       const void* __restrict__ xB, const void* __restrict__ kB, const void* __restrict__ vB, const void* __restrict__ wqB, const void* __restrict__ woB,
                                                       char* ws, __hip_bfloat16* __restrict__ out) {
  extern __shared__ __attribute__((aligned(16))) char smem_[];
  Ctx c;
  c.smem = (LAS char*)smem_; c.lds0 = (unsigned)(uintptr_t)c.smem;
  LAS char* smem = c.smem;
  const int lane = threadIdx.x & 63;
  const int wave = rfl(threadIdx.x >> 6), br = (wave >> 1) ^ (A_OLD ^ 1);
  TAKE(0);
  c.lane = lane; c.kh = wave & 1; c.ring = wave * 32768u;
  const int g8 = blockIdx.x & 7, jj = blockIdx.x >> 3, b = jj >> 2, head = 4 * g8 + (jj & 3);
  const size_t kvoff = (size_t)(br ? b * 32 + head : b * 8 + g8) * (SK * DH * 2);
  c.srdK = mk_rsrc((const char*)(br ? kB : kA) + kvoff, SK * DH * 2);
  c.srdV = mk_rsrc((const char*)(br ? vB : vA) + kvoff, SK * DH * 2);
  const rsrc_t srdW = mk_rsrc(br ? wqB : wqA, DM * NH * DH * 2);
  const rsrc_t srdX = mk_rsrc((const char*)(br ? xB : xA) + (size_t)b * (SQ * DM * 2), SQ * DM * 2);
#pragma unroll
  for (int i = 0; i < 4; ++i) c.vo[i] = (lane >> 4) * 256 + 16 * ((lane & 15) ^ (((lane >> 4) << 2) | i));

  bf16x8 qf[8];
  {
    f32x4 acc[8][2];
    if (br) qproj<NS_B, NT_B>(c, srdX, srdW, head, b, acc); else qproj<NS_A, NT_A>(c, srdX, srdW, head, b, acc);
    TAKE(1);
    LAS f32x4* mine = (LAS f32x4*)(smem + SCR + wave * 8192) + lane;
#pragma unroll
    for (int j = 0; j < 8; ++j) mine[j * 64] = acc[j][1];
    bar();
    LAS f32x4* other = (LAS f32x4*)(smem + SCR + (wave ^ 1) * 8192) + lane;
#pragma unroll
    for (int j = 0; j < 8; ++j) acc[j][0] += other[j * 64];
    bar();
    LAS char* qimg = smem + SCR + (wave >> 1) * 8192;
    const int cc = lane & 15, g = lane >> 4, q = cc + 16 * c.kh;
#pragma unroll
    for (int e = 0; e < 4; ++e)
      *(LAS u32x4*)(qimg + q * 256 + 16 * ((4 * g + e) ^ cc)) = u32x4{cvtpk(acc[0][0][e], acc[1][0][e]), cvtpk(acc[2][0][e], acc[3][0][e]), cvtpk(acc[4][0][e], acc[5][0][e]), cvtpk(acc[6][0][e], acc[7][0][e])};
    bar();
#pragma unroll
    for (int s = 0; s < 8; ++s) qf[s] = *(LAS bf16x8*)(qimg + (lane & 31) * 256 + 16 * ((2 * s + (lane >> 5)) ^ (lane & 15)));
  }
  const unsigned tag = *(const __attribute__((address_space(4))) unsigned*)(ws + WS_GEN) + 1u;
  GAS unsigned long long* outbox = (GAS unsigned long long*)(ws + WS_BOX) + (blockIdx.x ^ 2) * 1024 + threadIdx.x;
#pragma unroll
  for (int e = 0; e < 4; ++e) __hip_atomic_store(outbox + e * 256, (unsigned long long)(tag - 1u) << 32, __ATOMIC_RELAXED, __HIP_MEMORY_SCOPE_AGENT);
  TAKE(2);

  f32x16 o[4] = {};
  float m = -__builtin_inff(), l = 0.f;
  if (br) attn_loop<NS_B, NT_B>(c, qf, o, m, l); else attn_loop<NS_A, NT_A>(c, qf, o, m, l);
  TAKE(3);

  const int obr = g8 >> 2, okh = (g8 >> 1) & 1, ob = jj >> 2, ocb = 4 * (g8 & 1) + (jj & 3), orot = jj & 3, wrot = ob;
  const int h0 = okh * 16 + wave * 4;
  const rsrc_t srdWo = mk_rsrc(obr ? woB : woA, NH * DH * DM * 2);
  const unsigned wvo = (lane >> 3) * 1024 + 16 * ((lane & 7) ^ (((lane >> 4) & 1) << 2));
#define DMA_W(R, I0, I1) _Pragma("unroll") for (int i = (I0); i < (I1); ++i) \
    asm volatile("s_mov_b32 m0, %0\n\ts_nop 0\n\tbuffer_load_dwordx4 %1, %2, %3 offen lds" :: "s"(c.lds0 + c.ring + 1024u * i), "v"(wvo), "s"(srdWo), \
                 "s"((unsigned)((h0 + (((R) * 2 + (i >> 4) + orot) & 3)) * 128 + (i & 15) * 8) * 1024u + ocb * 128u) : "memory")
  GAS unsigned* gen = (GAS unsigned*)(ws + WS_GEN);
  GAS unsigned* flags = (GAS unsigned*)(ws + WS_FLAG);
  (void)wrot;

  const int r32 = lane & 31, hi = lane >> 5;
  { auto rr = __builtin_amdgcn_permlane32_swap(__float_as_uint(l), __float_as_uint(l), false, false); l = __uint_as_float(rr[0]) + __uint_as_float(rr[1]); }
  {
    LAS f32x4* mine = (LAS f32x4*)(smem + SCR + wave * 8192) + lane;
    LAS f32x4* other = (LAS f32x4*)(smem + SCR + (wave ^ 1) * 8192) + lane;
    if (hi == 0) *(LAS f32x2*)(smem + SCR + wave * 8192 + r32 * 8) = f32x2{m, l};
    bar();
    const f32x2 v = *(LAS f32x2*)(smem + SCR + (wave ^ 1) * 8192 + r32 * 8);
    bar();
    const float ms = fmaxf(m, v[0]), e0 = __builtin_amdgcn_exp2f((m - ms) * SC2), e1 = __builtin_amdgcn_exp2f((v[0] - ms) * SC2);
    const float L = c.kh ? fmaf(e1, v[1], e0 * l) : fmaf(e0, l, e1 * v[1]);
    const float f = e0 / L;
#pragma unroll
    for (int u = 0; u < 8; ++u) mine[u * 64] = f32x4{o[2 + (u >> 2)][4 * (u & 3)] * f, o[2 + (u >> 2)][4 * (u & 3) + 1] * f, o[2 + (u >> 2)][4 * (u & 3) + 2] * f, o[2 + (u >> 2)][4 * (u & 3) + 3] * f};
    bar();
    const rsrc_t srdOw = mk_rsrc(ws + WS_O + (size_t)((br * NB + b) * NH + head) * 8192, 8192);
#pragma unroll
    for (int u = 0; u < 4; ++u) {
      const f32x4 a0 = other[(2 * u) * 64], a1 = other[(2 * u + 1) * 64];
      const int cb = u >> 1, r0 = 8 * (u & 1);
      const u32x4 w = {cvtpk(fmaf(o[cb][r0], f, a0[0]), fmaf(o[cb][r0 + 1], f, a0[1])), cvtpk(fmaf(o[cb][r0 + 2], f, a0[2]), fmaf(o[cb][r0 + 3], f, a0[3])),
                       cvtpk(fmaf(o[cb][r0 + 4], f, a1[0]), fmaf(o[cb][r0 + 5], f, a1[1])), cvtpk(fmaf(o[cb][r0 + 6], f, a1[2]), fmaf(o[cb][r0 + 7], f, a1[3]))};
      __builtin_amdgcn_raw_buffer_store_b128(w, srdOw, lane * 16, (c.kh * 4 + u) * 1024, 16);
    }
  }
  vmwait<0>();
  bar();
  if (threadIdx.x < 2) __hip_atomic_store(flags + (threadIdx.x * NB + b) * NH + head, tag, __ATOMIC_RELAXED, __HIP_MEMORY_SCOPE_AGENT);
  TAKE(4);
  DMA_W(0, 0, 32);

  const int blk = (lane >> 4) & 1, q4 = (lane & 15) >> 2, p4 = lane & 3;
  const unsigned waddr = c.ring + 128 * (8 * hi + q4) + 64 * (q4 >> 1) + 32 * blk + 8 * p4;
  bf16x8 wf[16][2];
#define RD_W(S0, S1) _Pragma("unroll") for (int s = (S0); s < (S1); ++s) _Pragma("unroll") for (int nb = 0; nb < 2; ++nb) { \
    const bf16x4 lo = __builtin_amdgcn_ds_read_tr16_b64_v4bf16((LAS bf16x4*)(smem + (waddr ^ (64u * nb)) + 2048u * s)); \
    const bf16x4 up = __builtin_amdgcn_ds_read_tr16_b64_v4bf16((LAS bf16x4*)(smem + (waddr ^ (64u * nb)) + 2048u * s + 512u)); \
    wf[s][nb] = __builtin_shufflevector(lo, up, 0, 1, 2, 3, 4, 5, 6, 7); }
  vmwait<0>();
  RD_W(0, 16);
  asm volatile("s_waitcnt lgkmcnt(0)" ::: "memory");
  SBAR();
  DMA_W(1, 0, 32);
  {
    GAS unsigned* fp = flags + (obr * NB + ob) * NH + h0 + (lane & 3);
    for (unsigned spins = 0; spins < SPIN_CAP; ++spins) {
      if (__all(__hip_atomic_load(fp, __ATOMIC_RELAXED, __HIP_MEMORY_SCOPE_AGENT) == tag)) break;
      __builtin_amdgcn_s_sleep(1);
    }
    asm volatile("" ::: "memory");
  }
  TAKE(5);
  const rsrc_t srdO = mk_rsrc(ws + WS_O + (size_t)((obr * NB + ob) * NH + h0) * 8192, 4 * 8192);
  u32x4 of[32];
#define O_ISSUE(A) _Pragma("unroll") for (int s = (A); s < (A) + 8; ++s) of[s] = bload_sc1(srdO, lane * 16, (unsigned)((((s >> 3) + orot) & 3) * 8 + (s & 7)) * 1024u)
#define O_WAIT(S, N) asm volatile("s_waitcnt vmcnt(%8)" : "+v"(of[S]), "+v"(of[S + 1]), "+v"(of[S + 2]), "+v"(of[S + 3]), "+v"(of[S + 4]), "+v"(of[S + 5]), "+v"(of[S + 6]), "+v"(of[S + 7]) : "n"(N) : "memory")
#define O_MFMA(A) _Pragma("unroll") for (int s = (A); s < (A) + 8; ++s) _Pragma("unroll") for (int nb = 0; nb < 2; ++nb) \
    acc[nb] = __builtin_amdgcn_mfma_f32_32x32x16_bf16(wf[s & 15][nb], __builtin_bit_cast(bf16x8, of[s]), acc[nb], 0, 0, 0)
  f32x16 acc[2] = {};
  O_ISSUE(0); O_ISSUE(8);
  SBAR(); O_WAIT(0, 8); SBAR(); O_MFMA(0); SBAR();
  O_ISSUE(16); O_ISSUE(24);
  SBAR(); O_WAIT(8, 16); SBAR(); O_MFMA(8); SBAR();
  RD_W(0, 8);
  SBAR(); O_WAIT(16, 8); SBAR(); O_MFMA(16); SBAR();
  RD_W(8, 16);
  SBAR(); O_WAIT(24, 0); SBAR(); O_MFMA(24);
  TAKE(6);
  LAS f32x4* red = (LAS f32x4*)(smem + SCR);
#pragma unroll
  for (int u = 0; u < 8; ++u) red[(wave * 8 + u) * 64 + lane] = f32x4{acc[u >> 2][4 * (u & 3)], acc[u >> 2][4 * (u & 3) + 1], acc[u >> 2][4 * (u & 3) + 2], acc[u >> 2][4 * (u & 3) + 3]};
  bar();
  {
    f32x4 a = red[(okh * 4 + wave) * 64 + lane], p = red[((okh ^ 1) * 4 + wave) * 64 + lane];
#pragma unroll
    for (int w = 1; w < 4; ++w) { a += red[(w * 8 + okh * 4 + wave) * 64 + lane]; p += red[(w * 8 + (okh ^ 1) * 4 + wave) * 64 + lane]; }
#pragma unroll
    for (int e = 0; e < 4; ++e) __hip_atomic_store(outbox + e * 256, ((unsigned long long)tag << 32) | __float_as_uint(p[e]), __ATOMIC_RELAXED, __HIP_MEMORY_SCOPE_AGENT);
    GAS unsigned long long* inbox = (GAS unsigned long long*)(ws + WS_BOX) + blockIdx.x * 1024 + threadIdx.x;
    unsigned long long g[4];
    for (unsigned spins = 0; spins < SPIN_CAP; ++spins) {
      bool ok = true;
#pragma unroll
      for (int e = 0; e < 4; ++e) { g[e] = __hip_atomic_load(inbox + e * 256, __ATOMIC_RELAXED, __HIP_MEMORY_SCOPE_AGENT); ok &= (unsigned)(g[e] >> 32) == tag; }
      if (__all(ok)) break;
      __builtin_amdgcn_s_sleep(1);
    }
#pragma unroll
    for (int e = 0; e < 4; ++e) a[e] += __uint_as_float((unsigned)g[e]);
    *(u32x2*)(out + (size_t)(ob * SQ + r32) * 1024 + obr * 512 + ocb * 64 + okh * 32 + 8 * wave + 4 * hi) = u32x2{cvtpk(a[0], a[1]), cvtpk(a[2], a[3])};
  }
  if (blockIdx.x == 0 && wave == 0) {
    for (unsigned spins = 0; spins < SPIN_CAP; ++spins) {
      bool ok = true;
#pragma unroll
      for (int i = 0; i < 8; ++i) ok &= __hip_atomic_load(flags + i * 64 + lane, __ATOMIC_RELAXED, __HIP_MEMORY_SCOPE_AGENT) == tag;
      if (__all(ok)) break;
      __builtin_amdgcn_s_sleep(1);
    }
    if (lane == 0) __hip_atomic_store(gen, tag, __ATOMIC_RELAXED, __HIP_MEMORY_SCOPE_AGENT);
  }
  TAKE(7);
}

extern "C" void kernel_launch(void* const* d_in, const int* in_sizes, int n_in, void* d_out, int out_size, void* d_ws, size_t ws_size, hipStream_t stream) {
  (void)in_sizes; (void)n_in; (void)out_size; (void)ws_size;
  (void)hipFuncSetAttribute((const void*)fused_kernel, hipFuncAttributeMaxDynamicSharedMemorySize, LDS1);
  hipLaunchKernelGGL(fused_kernel, dim3(256), dim3(256), LDS1, stream, d_in[0], d_in[1], d_in[2], d_in[3], d_in[4], d_in[5], d_in[6], d_in[7], d_in[8], d_in[9],
                     (char*)d_ws, (__hip_bfloat16*)d_out);
}
```

```cpp
#define DIAGK 7
#define DIAGM 0
#define STAMPS
#define DIAG
#include <hip/hip_runtime.h>
#include <hip/hip_bf16.h>
#include <stdint.h>

#define DI __device__ __forceinline__
#define LAS __attribute__((address_space(3)))
#define GAS __attribute__((address_space(1)))

typedef __attribute__((ext_vector_type(8))) __bf16 bf16x8;
typedef __attribute__((ext_vector_type(4))) __bf16 bf16x4;
typedef __attribute__((ext_vector_type(2))) __bf16 bf16x2;
typedef __attribute__((ext_vector_type(2))) float f32x2;
typedef __attribute__((ext_vector_type(4))) float f32x4;
typedef __attribute__((ext_vector_type(16))) float f32x16;
typedef __attribute__((ext_vector_type(4))) unsigned u32x4;
typedef __attribute__((ext_vector_type(2))) unsigned u32x2;
typedef __amdgpu_buffer_rsrc_t rsrc_t;

constexpr int NB = 8, SQ = 32, DM = 512, NH = 32, SK = 2048, DH = 128;
constexpr int NT = 32;
constexpr unsigned SCR = 131072;
constexpr unsigned LDS1 = 163840;
constexpr float SC2 = 0.08838834764831845f * 1.4426950408889634f;
constexpr unsigned WS_GEN = 0, WS_FLAG = 4096, WS_O = 65536, WS_BOX = WS_O + (4u << 20);
constexpr unsigned SPIN_CAP = 1u << 17;

#define A_OLD 1
#define NS_A 2
#define NS_B 2
#define NT_A 0
#define NT_B 1
#define OMAP 0
#define QBUF 3
#define NSTAMP 8
#define NWAVE 4
#define STAMP_OFF (64u << 20)
#define TAKE(k) st_[k] = __builtin_amdgcn_s_memrealtime()

DI unsigned rfl(unsigned x) { return __builtin_amdgcn_readfirstlane(x); }
DI rsrc_t mk_rsrc(const void* p, unsigned bytes) { return __builtin_amdgcn_make_buffer_rsrc((void*)p, 0, bytes, 0x00020000); }
DI unsigned cvtpk(float lo, float hi) { f32x2 v = {lo, hi}; bf16x2 b = __builtin_convertvector(v, bf16x2); return __builtin_bit_cast(unsigned, b); }
DI void bar() { asm volatile("s_waitcnt lgkmcnt(0)\n\ts_barrier" ::: "memory"); }
template <int N> DI void vmwait() { asm volatile("s_waitcnt vmcnt(%0)" :: "n"(N) : "memory"); }
#define SBAR() __builtin_amdgcn_sched_barrier(0)

#define DMA_TILE(NTS) asm volatile( \
    "s_waitcnt lgkmcnt(0)\n\t" \
    "s_mov_b32 m0, %0\n\ts_nop 0\n\t" \
    "buffer_load_dwordx4 %5, %4, %2 offen" NTS " lds\n\t" \
    "buffer_load_dwordx4 %6, %4, %2 offen offset:1024" NTS " lds\n\t" \
    "buffer_load_dwordx4 %7, %4, %2 offen offset:2048" NTS " lds\n\t" \
    "buffer_load_dwordx4 %8, %4, %2 offen offset:3072" NTS " lds\n\t" \
    "s_mov_b32 m0, %1\n\ts_nop 0\n\t" \
    "buffer_load_dwordx4 %5, %4, %3 offen" NTS " lds\n\t" \
    "buffer_load_dwordx4 %6, %4, %3 offen offset:1024" NTS " lds\n\t" \
    "buffer_load_dwordx4 %7, %4, %3 offen offset:2048" NTS " lds\n\t" \
    "buffer_load_dwordx4 %8, %4, %3 offen offset:3072" NTS " lds" \
    :: "s"(lds), "s"(lds + 4096u), "s"(soff), "s"(soff + 4096u), "s"(srd), "v"(vo[0]), "v"(vo[1]), "v"(vo[2]), "v"(vo[3]) : "memory")
template <int NTP> DI void dma_tile(unsigned lds, rsrc_t srd, unsigned soff, const unsigned (&vo)[4]) {
  if constexpr (NTP) DMA_TILE(" nt"); else DMA_TILE("");
}
DI u32x4 bload(rsrc_t srd, unsigned voff, unsigned soff) {
  u32x4 r; asm volatile("buffer_load_dwordx4 %0, %1, %2, %3 offen" : "=v"(r) : "v"(voff), "s"(srd), "s"(soff) : "memory"); return r;
}
DI u32x4 bload_sc1(rsrc_t srd, unsigned voff, unsigned soff) {
  u32x4 r; asm volatile("buffer_load_dwordx4 %0, %1, %2, %3 offen sc1" : "=v"(r) : "v"(voff), "s"(srd), "s"(soff) : "memory"); return r;
}

struct Ctx { LAS char* smem; unsigned lds0, ring; rsrc_t srdK, srdV; unsigned vo[4]; int lane, kh; };

template <int NS, int NTP> DI void qproj(const Ctx& c, rsrc_t srdX, rsrc_t srdW, int head, int rot, f32x4 (&acc)[8][2]) {
  const int cc = c.lane & 15, g = c.lane >> 4;
  u32x4 xr[8][2], wr[8][8];
  const unsigned xv0 = (cc + 16 * c.kh) * 1024 + g * 16, xv1 = (cc + 16 * (c.kh ^ 1)) * 1024 + g * 16;
  const unsigned wvo = g * 65536 + 16 * cc;
#define QISSUE(ks) do { const unsigned kk = (unsigned)(c.kh * 8 + (((ks) + rot) & 7)); \
    xr[ks][0] = bload(srdX, xv0, kk * 64); xr[ks][1] = bload(srdX, xv1, kk * 64); \
    _Pragma("unroll") for (int i = 0; i < 8; ++i) wr[ks][i] = bload(srdW, wvo, (kk * 32 + i) * 8192 + head * 256); } while (0)
#define KVISSUE(j) do { if constexpr ((j) < NS) dma_tile<NTP>(c.lds0 + c.ring + (j) * 8192u, ((j) & 1) ? c.srdV : c.srdK, (unsigned)(((j) >> 1) * 2 + c.kh) * 8192u, c.vo); } while (0)
#pragma unroll
  for (int ks = 0; ks < QBUF; ++ks) QISSUE(ks);
#pragma unroll
  for (int ks = 0; ks < 8; ++ks) {
    SBAR();
    const int nq = (ks + QBUF - 1 < 7 ? ks + QBUF - 1 : 7) - ks;
    const int lo = ks - QBUF + 1 > 0 ? ks - QBUF + 1 : 0;
    const int hi = ks < NS ? ks : NS;
    const int nd = hi > lo ? hi - lo : 0;
    const int n = nq * 10 + nd * 8;
#define QW(N) asm volatile("s_waitcnt vmcnt(%10)" : "+v"(xr[ks][0]), "+v"(xr[ks][1]), "+v"(wr[ks][0]), "+v"(wr[ks][1]), "+v"(wr[ks][2]), "+v"(wr[ks][3]), \
                           "+v"(wr[ks][4]), "+v"(wr[ks][5]), "+v"(wr[ks][6]), "+v"(wr[ks][7]) : "n"(N) : "memory")
    QW(n);
    if (ks == 0) KVISSUE(0); if (ks == 1) KVISSUE(1); if (ks == 2) KVISSUE(2); if (ks == 3) KVISSUE(3);
    if (ks + QBUF < 8) QISSUE((ks + QBUF) & 7);
    SBAR();
#pragma unroll
    for (int j = 0; j < 8; ++j) {
      u32x4 f;
#pragma unroll
      for (int m = 0; m < 4; ++m) f[m] = __builtin_amdgcn_perm(wr[ks][2 * m + 1][j >> 1], wr[ks][2 * m][j >> 1], (j & 1) ? 0x07060302u : 0x05040100u);
#pragma unroll
      for (int qb = 0; qb < 2; ++qb)
        acc[j][qb] = __builtin_amdgcn_mfma_f32_16x16x32_bf16(__builtin_bit_cast(bf16x8, f), __builtin_bit_cast(bf16x8, xr[ks][qb]), ks ? acc[j][qb] : f32x4{0.f, 0.f, 0.f, 0.f}, 0, 0, 0);
    }
  }
#undef QW
#undef QISSUE
#undef KVISSUE
}

template <int NS, int NTP> DI void attn_loop(const Ctx& c, const bf16x8 (&qf)[8], f32x16 (&o)[4], float& m, float& l) {
  const int lane = c.lane, r32 = lane & 31, hi = lane >> 5;
  const unsigned kaddr = c.ring + 256 * r32 + 16 * (hi ^ (((r32 & 3) << 2) | ((r32 >> 2) & 3)));
  const int blk = (lane >> 4) & 1, q4 = (lane & 15) >> 2, p4 = lane & 3;
  const unsigned vaddr = c.ring + 8192 + 256 * (4 * hi + q4) + 64 * q4 + 16 * ((2 * blk + (p4 & 1)) ^ hi) + 8 * (p4 >> 1);
  const unsigned vx = c.kh * 128u;
  constexpr int LA = NS / 2;
#pragma unroll 1
  for (int t0 = 0; t0 < NT; t0 += LA) {
#pragma unroll
    for (int u = 0; u < LA; ++u) {
      const int t = t0 + u;
      const unsigned so = u * 16384u;
      const bool more = t + LA < NT;
      const unsigned soffn = (unsigned)((t + LA) * 2 + c.kh) * 8192u;
      if (more || u == 0) vmwait<(NS - 1) * 8>(); else vmwait<(NS - 3) * 8 < 0 ? 0 : (NS - 3) * 8>();
      bf16x8 kf[8];
#pragma unroll
      for (int s = 0; s < 8; ++s) kf[s] = *(LAS bf16x8*)(c.smem + (kaddr ^ (32u * s)) + so);
      if (more) dma_tile<NTP>(c.lds0 + c.ring + so, c.srdK, soffn, c.vo);
      f32x16 sc = {};
#pragma unroll
      for (int s = 0; s < 8; ++s) sc = __builtin_amdgcn_mfma_f32_32x32x16_bf16(kf[s], qf[s], sc, 0, 0, 0);
      float mx = sc[0];
#pragma unroll
      for (int i = 1; i < 16; ++i) mx = fmaxf(mx, sc[i]);
      { auto rr = __builtin_amdgcn_permlane32_swap(__float_as_uint(mx), __float_as_uint(mx), false, false); mx = fmaxf(__uint_as_float(rr[0]), __uint_as_float(rr[1])); }
      const float mn = fmaxf(m, mx), alpha = __builtin_amdgcn_exp2f((m - mn) * SC2), nm = -mn * SC2;
      m = mn;
      float ps = 0.f;
#pragma unroll
      for (int i = 0; i < 16; ++i) { sc[i] = __builtin_amdgcn_exp2f(fmaf(sc[i], SC2, nm)); ps += sc[i]; }
      l = fmaf(l, alpha, ps);
      bf16x8 pf[2];
#pragma unroll
      for (int s = 0; s < 2; ++s) pf[s] = __builtin_bit_cast(bf16x8, u32x4{cvtpk(sc[8 * s], sc[8 * s + 1]), cvtpk(sc[8 * s + 2], sc[8 * s + 3]), cvtpk(sc[8 * s + 4], sc[8 * s + 5]), cvtpk(sc[8 * s + 6], sc[8 * s + 7])});
#pragma unroll
      for (int cb = 0; cb < 4; ++cb) o[cb] *= alpha;
      if (more) vmwait<(NS - 1) * 8>(); else if (u == 0) vmwait<(NS - 2) * 8>(); else vmwait<0>();
      bf16x8 vf[4][2];
#pragma unroll
      for (int cb = 0; cb < 4; ++cb)
#pragma unroll
        for (int s = 0; s < 2; ++s) {
          const bf16x4 lo = __builtin_amdgcn_ds_read_tr16_b64_v4bf16((LAS bf16x4*)(c.smem + ((vaddr ^ vx ^ (64u * cb)) + 4096u * s + so)));
          const bf16x4 up = __builtin_amdgcn_ds_read_tr16_b64_v4bf16((LAS bf16x4*)(c.smem + ((vaddr ^ vx ^ (64u * cb) ^ 32u) + 4096u * s + 2048u + so)));
          vf[cb][s] = __builtin_shufflevector(lo, up, 0, 1, 2, 3, 4, 5, 6, 7);
        }
      if (more) dma_tile<NTP>(c.lds0 + c.ring + 8192u + so, c.srdV, soffn, c.vo);
#pragma unroll
      for (int s = 0; s < 2; ++s)
#pragma unroll
        for (int cb = 0; cb < 4; ++cb) o[cb] = __builtin_amdgcn_mfma_f32_32x32x16_bf16(vf[cb][s], pf[s], o[cb], 0, 0, 0);
    }
  }
}

__global__ __launch_bounds__(256, 2) void fused_kernel(const void* __restrict__ xA, const void* __restrict__ kA, const void* __restrict__ vA, const void* __restrict__ wqA, const void* __restrict__ woA,
                                                       const void* __restrict__ xB, const void* __restrict__ kB, const void* __restrict__ vB, const void* __restrict__ wqB, const void* __restrict__ woB,
                                                       char* ws, __hip_bfloat16* __restrict__ out) {
  extern __shared__ __attribute__((aligned(16))) char smem_[];
  Ctx c;
  c.smem = (LAS char*)smem_; c.lds0 = (unsigned)(uintptr_t)c.smem;
  LAS char* smem = c.smem;
  const int lane = threadIdx.x & 63;
  const int wave = rfl(threadIdx.x >> 6), br = (wave >> 1) ^ (A_OLD ^ 1);
  unsigned long long st_[NSTAMP];
  TAKE(0);
  c.lane = lane; c.kh = wave & 1; c.ring = wave * 32768u;
  const int g8 = blockIdx.x & 7, jj = blockIdx.x >> 3, b = jj >> 2, head = 4 * g8 + (jj & 3);
  const size_t kvoff = (size_t)(br ? b * 32 + head : b * 8 + g8) * (SK * DH * 2);
  c.srdK = mk_rsrc((const char*)(br ? kB : kA) + kvoff, SK * DH * 2);
  c.srdV = mk_rsrc((const char*)(br ? vB : vA) + kvoff, SK * DH * 2);
  const rsrc_t srdW = mk_rsrc(br ? wqB : wqA, DM * NH * DH * 2);
  const rsrc_t srdX = mk_rsrc((const char*)(br ? xB : xA) + (size_t)b * (SQ * DM * 2), SQ * DM * 2);
#pragma unroll
  for (int i = 0; i < 4; ++i) c.vo[i] = (lane >> 4) * 256 + 16 * ((lane & 15) ^ (((lane >> 4) << 2) | i));

  bf16x8 qf[8];
  {
    f32x4 acc[8][2];
    if (br) qproj<NS_B, NT_B>(c, srdX, srdW, head, b, acc); else qproj<NS_A, NT_A>(c, srdX, srdW, head, b, acc);
    TAKE(1);
    LAS f32x4* mine = (LAS f32x4*)(smem + SCR + wave * 8192) + lane;
#pragma unroll
    for (int j = 0; j < 8; ++j) mine[j * 64] = acc[j][1];
    bar();
    LAS f32x4* other = (LAS f32x4*)(smem + SCR + (wave ^ 1) * 8192) + lane;
#pragma unroll
    for (int j = 0; j < 8; ++j) acc[j][0] += other[j * 64];
    bar();
    LAS char* qimg = smem + SCR + (wave >> 1) * 8192;
    const int cc = lane & 15, g = lane >> 4, q = cc + 16 * c.kh;
#pragma unroll
    for (int e = 0; e < 4; ++e)
      *(LAS u32x4*)(qimg + q * 256 + 16 * ((4 * g + e) ^ cc)) = u32x4{cvtpk(acc[0][0][e], acc[1][0][e]), cvtpk(acc[2][0][e], acc[3][0][e]), cvtpk(acc[4][0][e], acc[5][0][e]), cvtpk(acc[6][0][e], acc[7][0][e])};
    bar();
#pragma unroll
    for (int s = 0; s < 8; ++s) qf[s] = *(LAS bf16x8*)(qimg + (lane & 31) * 256 + 16 * ((2 * s + (lane >> 5)) ^ (lane & 15)));
  }
  const unsigned tag = *(const __attribute__((address_space(4))) unsigned*)(ws + WS_GEN) + 1u;
  GAS unsigned long long* outbox = (GAS unsigned long long*)(ws + WS_BOX) + (blockIdx.x ^ 2) * 1024 + threadIdx.x;
#pragma unroll
  for (int e = 0; e < 4; ++e) __hip_atomic_store(outbox + e * 256, (unsigned long long)(tag - 1u) << 32, __ATOMIC_RELAXED, __HIP_MEMORY_SCOPE_AGENT);
  TAKE(2);

  f32x16 o[4] = {};
  float m = -__builtin_inff(), l = 0.f;
  if (br) attn_loop<NS_B, NT_B>(c, qf, o, m, l); else attn_loop<NS_A, NT_A>(c, qf, o, m, l);
  TAKE(3);
  if (lane == 0) for (int k = 0; k < 4; ++k) __hip_atomic_store((GAS unsigned long long*)(ws + STAMP_OFF) + (blockIdx.x * 4 + wave) * NSTAMP + k, st_[k], __ATOMIC_RELAXED, __HIP_MEMORY_SCOPE_AGENT);

  const int obr = g8 >> 2, okh = (g8 >> 1) & 1, ob = jj >> 2, ocb = 4 * (g8 & 1) + (jj & 3), orot = jj & 3, wrot = ob;
  const int h0 = okh * 16 + wave * 4;
  const rsrc_t srdWo = mk_rsrc(obr ? woB : woA, NH * DH * DM * 2);
  const unsigned wvo = (lane >> 3) * 1024 + 16 * ((lane & 7) ^ (((lane >> 4) & 1) << 2));
#define DMA_W(R, I0, I1) _Pragma("unroll") for (int i = (I0); i < (I1); ++i) \
    asm volatile("s_mov_b32 m0, %0\n\ts_nop 0\n\tbuffer_load_dwordx4 %1, %2, %3 offen lds" :: "s"(c.lds0 + c.ring + 1024u * i), "v"(wvo), "s"(srdWo), \
                 "s"((unsigned)((h0 + (((R) * 2 + (i >> 4) + orot) & 3)) * 128 + (i & 15) * 8) * 1024u + ocb * 128u) : "memory")
  GAS unsigned* gen = (GAS unsigned*)(ws + WS_GEN);
  GAS unsigned* flags = (GAS unsigned*)(ws + WS_FLAG);
  (void)wrot;

  const int r32 = lane & 31, hi = lane >> 5;
  { auto rr = __builtin_amdgcn_permlane32_swap(__float_as_uint(l), __float_as_uint(l), false, false); l = __uint_as_float(rr[0]) + __uint_as_float(rr[1]); }
  {
    LAS f32x4* mine = (LAS f32x4*)(smem + SCR + wave * 8192) + lane;
    LAS f32x4* other = (LAS f32x4*)(smem + SCR + (wave ^ 1) * 8192) + lane;
    if (hi == 0) *(LAS f32x2*)(smem + SCR + wave * 8192 + r32 * 8) = f32x2{m, l};
    bar();
    const f32x2 v = *(LAS f32x2*)(smem + SCR + (wave ^ 1) * 8192 + r32 * 8);
    bar();
    const float ms = fmaxf(m, v[0]), e0 = __builtin_amdgcn_exp2f((m - ms) * SC2), e1 = __builtin_amdgcn_exp2f((v[0] - ms) * SC2);
    const float L = c.kh ? fmaf(e1, v[1], e0 * l) : fmaf(e0, l, e1 * v[1]);
    const float f = e0 / L;
#pragma unroll
    for (int u = 0; u < 8; ++u) mine[u * 64] = f32x4{o[2 + (u >> 2)][4 * (u & 3)] * f, o[2 + (u >> 2)][4 * (u & 3) + 1] * f, o[2 + (u >> 2)][4 * (u & 3) + 2] * f, o[2 + (u >> 2)][4 * (u & 3) + 3] * f};
    bar();
    const rsrc_t srdOw = mk_rsrc(ws + WS_O + (size_t)((br * NB + b) * NH + head) * 8192, 8192);
#pragma unroll
    for (int u = 0; u < 4; ++u) {
      const f32x4 a0 = other[(2 * u) * 64], a1 = other[(2 * u + 1) * 64];
      const int cb = u >> 1, r0 = 8 * (u & 1);
      const u32x4 w = {cvtpk(fmaf(o[cb][r0], f, a0[0]), fmaf(o[cb][r0 + 1], f, a0[1])), cvtpk(fmaf(o[cb][r0 + 2], f, a0[2]), fmaf(o[cb][r0 + 3], f, a0[3])),
                       cvtpk(fmaf(o[cb][r0 + 4], f, a1[0]), fmaf(o[cb][r0 + 5], f, a1[1])), cvtpk(fmaf(o[cb][r0 + 6], f, a1[2]), fmaf(o[cb][r0 + 7], f, a1[3]))};
      __builtin_amdgcn_raw_buffer_store_b128(w, srdOw, lane * 16, (c.kh * 4 + u) * 1024, 16);
    }
  }
  vmwait<0>();
  bar();
  if (threadIdx.x < 2) __hip_atomic_store(flags + (threadIdx.x * NB + b) * NH + head, tag, __ATOMIC_RELAXED, __HIP_MEMORY_SCOPE_AGENT);
  TAKE(4);
  DMA_W(0, 0, 32);

  const int blk = (lane >> 4) & 1, q4 = (lane & 15) >> 2, p4 = lane & 3;
  const unsigned waddr = c.ring + 128 * (8 * hi + q4) + 64 * (q4 >> 1) + 32 * blk + 8 * p4;
  bf16x8 wf[16][2];
#define RD_W(S0, S1) _Pragma("unroll") for (int s = (S0); s < (S1); ++s) _Pragma("unroll") for (int nb = 0; nb < 2; ++nb) { \
    const bf16x4 lo = __builtin_amdgcn_ds_read_tr16_b64_v4bf16((LAS bf16x4*)(smem + (waddr ^ (64u * nb)) + 2048u * s)); \
    const bf16x4 up = __builtin_amdgcn_ds_read_tr16_b64_v4bf16((LAS bf16x4*)(smem + (waddr ^ (64u * nb)) + 2048u * s + 512u)); \
    wf[s][nb] = __builtin_shufflevector(lo, up, 0, 1, 2, 3, 4, 5, 6, 7); }
  vmwait<0>();
  RD_W(0, 16);
  asm volatile("s_waitcnt lgkmcnt(0)" ::: "memory");
  SBAR();
  DMA_W(1, 0, 32);
  {
    GAS unsigned* fp = flags + (obr * NB + ob) * NH + h0 + (lane & 3);
    for (unsigned spins = 0; spins < SPIN_CAP; ++spins) {
      if (__all(__hip_atomic_load(fp, __ATOMIC_RELAXED, __HIP_MEMORY_SCOPE_AGENT) == tag)) break;
      __builtin_amdgcn_s_sleep(1);
    }
    asm volatile("" ::: "memory");
  }
  TAKE(5);
  const rsrc_t srdO = mk_rsrc(ws + WS_O + (size_t)((obr * NB + ob) * NH + h0) * 8192, 4 * 8192);
  u32x4 of[32];
#define O_ISSUE(A) _Pragma("unroll") for (int s = (A); s < (A) + 8; ++s) of[s] = bload_sc1(srdO, lane * 16, (unsigned)((((s >> 3) + orot) & 3) * 8 + (s & 7)) * 1024u)
#define O_WAIT(S, N) asm volatile("s_waitcnt vmcnt(%8)" : "+v"(of[S]), "+v"(of[S + 1]), "+v"(of[S + 2]), "+v"(of[S + 3]), "+v"(of[S + 4]), "+v"(of[S + 5]), "+v"(of[S + 6]), "+v"(of[S + 7]) : "n"(N) : "memory")
#define O_MFMA(A) _Pragma("unroll") for (int s = (A); s < (A) + 8; ++s) _Pragma("unroll") for (int nb = 0; nb < 2; ++nb) \
    acc[nb] = __builtin_amdgcn_mfma_f32_32x32x16_bf16(wf[s & 15][nb], __builtin_bit_cast(bf16x8, of[s]), acc[nb], 0, 0, 0)
  f32x16 acc[2] = {};
  O_ISSUE(0); O_ISSUE(8);
  SBAR(); O_WAIT(0, 8); SBAR(); O_MFMA(0); SBAR();
  O_ISSUE(16); O_ISSUE(24);
  SBAR(); O_WAIT(8, 16); SBAR(); O_MFMA(8); SBAR();
  RD_W(0, 8);
  SBAR(); O_WAIT(16, 8); SBAR(); O_MFMA(16); SBAR();
  RD_W(8, 16);
  SBAR(); O_WAIT(24, 0); SBAR(); O_MFMA(24);
  TAKE(6);
  LAS f32x4* red = (LAS f32x4*)(smem + SCR);
#pragma unroll
  for (int u = 0; u < 8; ++u) red[(wave * 8 + u) * 64 + lane] = f32x4{acc[u >> 2][4 * (u & 3)], acc[u >> 2][4 * (u & 3) + 1], acc[u >> 2][4 * (u & 3) + 2], acc[u >> 2][4 * (u & 3) + 3]};
  bar();
  {
    f32x4 a = red[(okh * 4 + wave) * 64 + lane], p = red[((okh ^ 1) * 4 + wave) * 64 + lane];
#pragma unroll
    for (int w = 1; w < 4; ++w) { a += red[(w * 8 + okh * 4 + wave) * 64 + lane]; p += red[(w * 8 + (okh ^ 1) * 4 + wave) * 64 + lane]; }
#pragma unroll
    for (int e = 0; e < 4; ++e) __hip_atomic_store(outbox + e * 256, ((unsigned long long)tag << 32) | __float_as_uint(p[e]), __ATOMIC_RELAXED, __HIP_MEMORY_SCOPE_AGENT);
    GAS unsigned long long* inbox = (GAS unsigned long long*)(ws + WS_BOX) + blockIdx.x * 1024 + threadIdx.x;
    unsigned long long g[4];
    for (unsigned spins = 0; spins < SPIN_CAP; ++spins) {
      bool ok = true;
#pragma unroll
      for (int e = 0; e < 4; ++e) { g[e] = __hip_atomic_load(inbox + e * 256, __ATOMIC_RELAXED, __HIP_MEMORY_SCOPE_AGENT); ok &= (unsigned)(g[e] >> 32) == tag; }
      if (__all(ok)) break;
      __builtin_amdgcn_s_sleep(1);
    }
#pragma unroll
    for (int e = 0; e < 4; ++e) a[e] += __uint_as_float((unsigned)g[e]);
    *(u32x2*)(out + (size_t)(ob * SQ + r32) * 1024 + obr * 512 + ocb * 64 + okh * 32 + 8 * wave + 4 * hi) = u32x2{cvtpk(a[0], a[1]), cvtpk(a[2], a[3])};
  }
  if (blockIdx.x == 0 && wave == 0) {
    for (unsigned spins = 0; spins < SPIN_CAP; ++spins) {
      bool ok = true;
#pragma unroll
      for (int i = 0; i < 8; ++i) ok &= __hip_atomic_load(flags + i * 64 + lane, __ATOMIC_RELAXED, __HIP_MEMORY_SCOPE_AGENT) == tag;
      if (__all(ok)) break;
      __builtin_amdgcn_s_sleep(1);
    }
    if (lane == 0) __hip_atomic_store(gen, tag, __ATOMIC_RELAXED, __HIP_MEMORY_SCOPE_AGENT);
  }
  TAKE(7);
  if (blockIdx.x == 0 && wave == 0) {
    GAS unsigned long long* st = (GAS unsigned long long*)(ws + STAMP_OFF);
    unsigned long long mn = ~0ull, mk = 0;
    for (int w = lane; w < 1024; w += 64) { const unsigned long long a = __hip_atomic_load(st + w * NSTAMP, __ATOMIC_RELAXED, __HIP_MEMORY_SCOPE_AGENT), e = __hip_atomic_load(st + w * NSTAMP + (DIAGK & 3), __ATOMIC_RELAXED, __HIP_MEMORY_SCOPE_AGENT);
      mn = a < mn ? a : mn; mk = e > mk ? e : mk; }
    for (int d = 1; d < 64; d <<= 1) { unsigned long long a = __shfl_xor(mn, d), e = __shfl_xor(mk, d); mn = a < mn ? a : mn; mk = e > mk ? e : mk; }
    const unsigned long long X = DIAGK < 4 ? mk - mn : st_[DIAGK] - mn;
    const unsigned long long t1_ = __builtin_amdgcn_s_memrealtime(), until = t1_ + DIAGM * (X > 100000 ? 100000 : X); while (__builtin_amdgcn_s_memrealtime() < until) __builtin_amdgcn_s_sleep(2);
  }
}

extern "C" void kernel_launch(void* const* d_in, const int* in_sizes, int n_in, void* d_out, int out_size, void* d_ws, size_t ws_size, hipStream_t stream) {
  (void)in_sizes; (void)n_in; (void)out_size; (void)ws_size;
  (void)hipFuncSetAttribute((const void*)fused_kernel, hipFuncAttributeMaxDynamicSharedMemorySize, LDS1);
  hipLaunchKernelGGL(fused_kernel, dim3(256), dim3(256), LDS1, stream, d_in[0], d_in[1], d_in[2], d_in[3], d_in[4], d_in[5], d_in[6], d_in[7], d_in[8], d_in[9],
                     (char*)d_ws, (__hip_bfloat16*)d_out);
}
```

```cpp
#include <hip/hip_runtime.h>
#include <hip/hip_bf16.h>
#include <stdint.h>

#define DI __device__ __forceinline__
#define LAS __attribute__((address_space(3)))
#define GAS __attribute__((address_space(1)))

typedef __attribute__((ext_vector_type(8))) __bf16 bf16x8;
typedef __attribute__((ext_vector_type(4))) __bf16 bf16x4;
typedef __attribute__((ext_vector_type(2))) __bf16 bf16x2;
typedef __attribute__((ext_vector_type(2))) float f32x2;
typedef __attribute__((ext_vector_type(4))) float f32x4;
typedef __attribute__((ext_vector_type(16))) float f32x16;
typedef __attribute__((ext_vector_type(4))) unsigned u32x4;
typedef __attribute__((ext_vector_type(2))) unsigned u32x2;
typedef __amdgpu_buffer_rsrc_t rsrc_t;

constexpr int NB = 8, SQ = 32, DM = 512, NH = 32, SK = 2048, DH = 128;
constexpr int NT = 32;
constexpr unsigned SCR = 131072;
constexpr unsigned LDS1 = 163840;
constexpr float SC2 = 0.08838834764831845f * 1.4426950408889634f;
constexpr unsigned WS_GEN = 0, WS_FLAG = 4096, WS_O = 65536, WS_BOX = WS_O + (4u << 20);
constexpr unsigned SPIN_CAP = 1u << 17;

#define A_OLD 1
#define NS_A 2
#define NS_B 2
#define NT_A 0
#define NT_B 1
#define OMAP 0
#define FTWAIT 0
#define QSCHED 1
#define PSLEEP 1
#define FSLEEP 1
#define NOBOX 0
#define PRECLR 0
#define OROT ((ob + ocb) & 7)
#define WPRE 0
#define WMID 0
#define WROT (ocb + (ob >> 1))
#define BOXPOLL 1
#define KPOS 5
#define VPOS 7
#define TROT 0
#define PRETOUCH 0
#define QBUF 3
#define TAKE(k)

DI unsigned rfl(unsigned x) { return __builtin_amdgcn_readfirstlane(x); }
DI rsrc_t mk_rsrc(const void* p, unsigned bytes) { return __builtin_amdgcn_make_buffer_rsrc((void*)p, 0, bytes, 0x00020000); }
DI unsigned cvtpk(float lo, float hi) { f32x2 v = {lo, hi}; bf16x2 b = __builtin_convertvector(v, bf16x2); return __builtin_bit_cast(unsigned, b); }
DI void bar() { asm volatile("s_waitcnt lgkmcnt(0)\n\ts_barrier" ::: "memory"); }
template <int N> DI void vmwait() { asm volatile("s_waitcnt vmcnt(%0)" :: "n"(N) : "memory"); }
#define ESTAMP(k)
#define SBAR() __builtin_amdgcn_sched_barrier(0)
#define SEGT(i)

#define DMA_TILE(NTS) asm volatile( \
    "s_waitcnt lgkmcnt(0)\n\t" \
    "s_mov_b32 m0, %0\n\ts_nop 0\n\t" \
    "buffer_load_dwordx4 %5, %4, %2 offen" NTS " lds\n\t" \
    "buffer_load_dwordx4 %6, %4, %2 offen offset:1024" NTS " lds\n\t" \
    "buffer_load_dwordx4 %7, %4, %2 offen offset:2048" NTS " lds\n\t" \
    "buffer_load_dwordx4 %8, %4, %2 offen offset:3072" NTS " lds\n\t" \
    "s_mov_b32 m0, %1\n\ts_nop 0\n\t" \
    "buffer_load_dwordx4 %5, %4, %3 offen" NTS " lds\n\t" \
    "buffer_load_dwordx4 %6, %4, %3 offen offset:1024" NTS " lds\n\t" \
    "buffer_load_dwordx4 %7, %4, %3 offen offset:2048" NTS " lds\n\t" \
    "buffer_load_dwordx4 %8, %4, %3 offen offset:3072" NTS " lds" \
    :: "s"(lds), "s"(lds + 4096u), "s"(soff), "s"(soff + 4096u), "s"(srd), "v"(vo[0]), "v"(vo[1]), "v"(vo[2]), "v"(vo[3]) : "memory")
template <int NTP> DI void dma_tile(unsigned lds, rsrc_t srd, unsigned soff, const unsigned (&vo)[4]) {
  if constexpr (NTP) DMA_TILE(" nt"); else DMA_TILE("");
}
DI u32x4 bload(rsrc_t srd, unsigned voff, unsigned soff) {
  u32x4 r; asm volatile("buffer_load_dwordx4 %0, %1, %2, %3 offen" : "=v"(r) : "v"(voff), "s"(srd), "s"(soff) : "memory"); return r;
}
DI u32x4 bload_sc1(rsrc_t srd, unsigned voff, unsigned soff) {
  u32x4 r; asm volatile("buffer_load_dwordx4 %0, %1, %2, %3 offen sc1" : "=v"(r) : "v"(voff), "s"(srd), "s"(soff) : "memory"); return r;
}

struct Ctx { LAS char* smem; unsigned lds0, ring; rsrc_t srdK, srdV, srdWo; unsigned vo[4], wvo, wso, tile0; int lane, kh, orot;
};

template <int NS, int NTP> DI void qproj(const Ctx& c, rsrc_t srdX, rsrc_t srdW, int head, int xrot, int wrot, f32x4 (&acc)[8][2]) {
  const int cc = c.lane & 15, g = c.lane >> 4;
  u32x4 wr[8][8];
  const unsigned wvo = g * 65536 + 16 * cc;
  const unsigned xvo = (c.lane >> 5) * 1024 + 16 * ((c.lane & 31) ^ (c.lane >> 5));
  const unsigned ximg = c.ring + 16384;
#define XISSUE(I0, I1) _Pragma("unroll") for (int i = (I0); i < (I1); ++i) { const unsigned p = (unsigned)(i + 4 * xrot) & 15u; \
    asm volatile("s_mov_b32 m0, %0\n\ts_nop 0\n\tbuffer_load_dwordx4 %1, %2, %3 offen lds" :: "s"(c.lds0 + ximg + 1024u * p), "v"(xvo ^ (32u * (p & 7u))), "s"(srdX), "s"(p * 2048u + c.kh * 512u) : "memory"); }
#define QISSUE(ks) do { const unsigned kk = (unsigned)(c.kh * 8 + (((ks) + wrot) & 7)); \
    _Pragma("unroll") for (int i = 0; i < 8; ++i) wr[ks][i] = bload(srdW, wvo, (kk * 32 + i) * 8192 + head * 256); } while (0)
  XISSUE(0, 4); QISSUE(0);
  XISSUE(4, 16);
#pragma unroll
  for (int ks = 1; ks < QBUF; ++ks) QISSUE(ks);
#pragma unroll
  for (int ks = 0; ks < 8; ++ks) {
    SBAR();
    const int nq = (ks + QBUF - 1 < 7 ? ks + QBUF - 1 : 7) - ks;
    const int n = nq * 8 + (ks > KPOS ? 8 : 0) + (ks > VPOS ? 8 : 0);
    asm volatile("s_waitcnt vmcnt(%8)" : "+v"(wr[ks][0]), "+v"(wr[ks][1]), "+v"(wr[ks][2]), "+v"(wr[ks][3]), "+v"(wr[ks][4]), "+v"(wr[ks][5]), "+v"(wr[ks][6]), "+v"(wr[ks][7]) : "n"(n) : "memory");
    if (ks + QBUF < 8) QISSUE((ks + QBUF) & 7);
    SBAR();
    bf16x8 xf[2];
    const unsigned ch = 4u * ((ks + wrot) & 7) + g;
#pragma unroll
    for (int qb = 0; qb < 2; ++qb) xf[qb] = *(LAS bf16x8*)(c.smem + ximg + (cc + 16 * (qb ^ c.kh)) * 512 + 16 * (ch ^ cc));
#pragma unroll
    for (int j = 0; j < 8; ++j) {
      u32x4 f;
#pragma unroll
      for (int m = 0; m < 4; ++m) f[m] = __builtin_amdgcn_perm(wr[ks][2 * m + 1][j >> 1], wr[ks][2 * m][j >> 1], (j & 1) ? 0x07060302u : 0x05040100u);
#pragma unroll
      for (int qb = 0; qb < 2; ++qb)
        acc[j][qb] = __builtin_amdgcn_mfma_f32_16x16x32_bf16(__builtin_bit_cast(bf16x8, f), xf[qb], ks ? acc[j][qb] : f32x4{0.f, 0.f, 0.f, 0.f}, 0, 0, 0);
    }
    __builtin_amdgcn_sched_group_barrier(0x100, 2, 0);
    __builtin_amdgcn_sched_group_barrier(0x2, 8, 0);
#pragma unroll
    for (int j = 0; j < 12; ++j) { __builtin_amdgcn_sched_group_barrier(0x8, 1, 0); __builtin_amdgcn_sched_group_barrier(0x2, 2, 0); }
    __builtin_amdgcn_sched_group_barrier(0x8, 4, 0);
    SBAR();
    if (ks == KPOS) dma_tile<NTP>(c.lds0 + c.ring, c.srdK, c.tile0 * 16384u + c.kh * 8192u, c.vo);
    if (ks == VPOS) dma_tile<NTP>(c.lds0 + c.ring + 8192u, c.srdV, c.tile0 * 16384u + c.kh * 8192u, c.vo);
  }
#undef XISSUE
#undef QISSUE
}

#define DMA_W(I0, I1) _Pragma("unroll") for (int i = (I0); i < (I1); ++i) \
    asm volatile("s_mov_b32 m0, %0\n\ts_nop 0\n\tbuffer_load_dwordx4 %1, %2, %3 offen lds" :: "s"(c.lds0 + c.ring + 1024u * ((i + 16) & 31)), "v"(c.wvo), "s"(c.srdWo), \
                 "s"((unsigned)((((i >> 2) + c.orot) & 7) * 32 + (i & 3) * 8) * 1024u + c.wso) : "memory")

template <int NS, int NTP> DI void attn_loop(const Ctx& c, const bf16x8 (&qf)[8], f32x16 (&o)[4], float& m, float& l) {
  const int lane = c.lane, r32 = lane & 31, hi = lane >> 5;
  const unsigned kaddr = c.ring + 256 * r32 + 16 * (hi ^ (((r32 & 3) << 2) | ((r32 >> 2) & 3)));
  const int blk = (lane >> 4) & 1, q4 = (lane & 15) >> 2, p4 = lane & 3;
  const unsigned vaddr = c.ring + 8192 + 256 * (4 * hi + q4) + 64 * q4 + 16 * ((2 * blk + (p4 & 1)) ^ hi) + 8 * (p4 >> 1);
  const unsigned vx = c.kh * 128u;
  static_assert(NS == 2, "ring");
  constexpr int LA = NS / 2;
#pragma unroll 1
  for (int t0 = 0; t0 < NT; t0 += LA) {
#pragma unroll
    for (int u = 0; u < LA; ++u) {
      const int t = t0 + u;
      const unsigned so = u * 16384u;
      const bool more = t + LA < NT;
      const unsigned soffn = (((t + LA + c.tile0) & (NT - 1)) * 2 + c.kh) * 8192u;
      SEGT(0);
      vmwait<8>();
      SEGT(1);
      bf16x8 kf[8];
#pragma unroll
      for (int s = 0; s < 8; ++s) kf[s] = *(LAS bf16x8*)(c.smem + (kaddr ^ (32u * s)) + so);
      SEGT(2);
      if (more) dma_tile<NTP>(c.lds0 + c.ring + so, c.srdK, soffn, c.vo);
      SEGT(3);
      f32x16 sc = {};
#pragma unroll
      for (int s = 0; s < 8; ++s) sc = __builtin_amdgcn_mfma_f32_32x32x16_bf16(kf[s], qf[s], sc, 0, 0, 0);
      float mx = sc[0];
#pragma unroll
      for (int i = 1; i < 16; ++i) mx = fmaxf(mx, sc[i]);
      { auto rr = __builtin_amdgcn_permlane32_swap(__float_as_uint(mx), __float_as_uint(mx), false, false); mx = fmaxf(__uint_as_float(rr[0]), __uint_as_float(rr[1])); }
      const float mn = fmaxf(m, mx), alpha = __builtin_amdgcn_exp2f((m - mn) * SC2), nm = -mn * SC2;
      m = mn;
      float ps = 0.f;
#pragma unroll
      for (int i = 0; i < 16; ++i) { sc[i] = __builtin_amdgcn_exp2f(fmaf(sc[i], SC2, nm)); ps += sc[i]; }
      l = fmaf(l, alpha, ps);
      bf16x8 pf[2];
#pragma unroll
      for (int s = 0; s < 2; ++s) pf[s] = __builtin_bit_cast(bf16x8, u32x4{cvtpk(sc[8 * s], sc[8 * s + 1]), cvtpk(sc[8 * s + 2], sc[8 * s + 3]), cvtpk(sc[8 * s + 4], sc[8 * s + 5]), cvtpk(sc[8 * s + 6], sc[8 * s + 7])});
#pragma unroll
      for (int cb = 0; cb < 4; ++cb) o[cb] *= alpha;
      SEGT(4);
      if (more) vmwait<8>(); else vmwait<0>();
      SEGT(5);
      bf16x8 vf[4][2];
#pragma unroll
      for (int cb = 0; cb < 4; ++cb)
#pragma unroll
        for (int s = 0; s < 2; ++s) {
          const bf16x4 lo = __builtin_amdgcn_ds_read_tr16_b64_v4bf16((LAS bf16x4*)(c.smem + ((vaddr ^ vx ^ (64u * cb)) + 4096u * s + so)));
          const bf16x4 up = __builtin_amdgcn_ds_read_tr16_b64_v4bf16((LAS bf16x4*)(c.smem + ((vaddr ^ vx ^ (64u * cb) ^ 32u) + 4096u * s + 2048u + so)));
          vf[cb][s] = __builtin_shufflevector(lo, up, 0, 1, 2, 3, 4, 5, 6, 7);
        }
      SEGT(6);
      if (more) dma_tile<NTP>(c.lds0 + c.ring + 8192u + so, c.srdV, soffn, c.vo);
      SEGT(7);
#pragma unroll
      for (int s = 0; s < 2; ++s)
#pragma unroll
        for (int cb = 0; cb < 4; ++cb) o[cb] = __builtin_amdgcn_mfma_f32_32x32x16_bf16(vf[cb][s], pf[s], o[cb], 0, 0, 0);
    }
  }
}

__global__ __launch_bounds__(256, 2) void fused_kernel(const void* __restrict__ xA, const void* __restrict__ kA, const void* __restrict__ vA, const void* __restrict__ wqA, const void* __restrict__ woA,
                                                       const void* __restrict__ xB, const void* __restrict__ kB, const void* __restrict__ vB, const void* __restrict__ wqB, const void* __restrict__ woB,
                                                       char* ws, __hip_bfloat16* __restrict__ out) {
  extern __shared__ __attribute__((aligned(16))) char smem_[];
  Ctx c;
  c.smem = (LAS char*)smem_; c.lds0 = (unsigned)(uintptr_t)c.smem;
  LAS char* smem = c.smem;
  const int lane = threadIdx.x & 63;
  const int wave = rfl(threadIdx.x >> 6), br = (wave >> 1) ^ (A_OLD ^ 1);
  TAKE(0);
  c.lane = lane; c.kh = wave & 1; c.ring = wave * 32768u;
  const int g8 = blockIdx.x & 7, jj = blockIdx.x >> 3, b = jj >> 2, head = 4 * g8 + (jj & 3);
  const size_t kvoff = (size_t)(br ? b * 32 + head : b * 8 + g8) * (SK * DH * 2);
  c.srdK = mk_rsrc((const char*)(br ? kB : kA) + kvoff, SK * DH * 2);
  c.srdV = mk_rsrc((const char*)(br ? vB : vA) + kvoff, SK * DH * 2);
  const rsrc_t srdW = mk_rsrc(br ? wqB : wqA, DM * NH * DH * 2);
  const rsrc_t srdX = mk_rsrc((const char*)(br ? xB : xA) + (size_t)b * (SQ * DM * 2), SQ * DM * 2);
#pragma unroll
  for (int i = 0; i < 4; ++i) c.vo[i] = (lane >> 4) * 256 + 16 * ((lane & 15) ^ (((lane >> 4) << 2) | i));

  c.tile0 = TROT == 0 ? 0u : TROT == 1 ? (unsigned)((br ? blockIdx.x * 11 : (b * 8 + g8) * 5 + 2) & 31) : (unsigned)(((br ? blockIdx.x : b * 8 + g8 + 300) * 2654435761u) >> 27);
  const int obr = g8 >> 2, okq = g8 & 3, ob = 2 * (jj >> 3), ocb = jj & 7, h0 = okq * 8 + ((wave + WROT) & 3) * 2;
  c.orot = OROT;
  c.srdWo = mk_rsrc(obr ? woB : woA, NH * DH * DM * 2);
  c.wvo = (lane >> 3) * 1024 + 16 * ((lane & 7) ^ (((lane >> 4) & 1) << 2));
  c.wso = h0 * 128 * 1024 + ocb * 128;
  bf16x8 qf[8];
  {
    f32x4 acc[8][2];
    if (br) qproj<NS_B, NT_B>(c, srdX, srdW, head, jj & 3, b, acc); else qproj<NS_A, NT_A>(c, srdX, srdW, head, jj & 3, b, acc);
    TAKE(1);
    LAS f32x4* mine = (LAS f32x4*)(smem + SCR + wave * 8192) + lane;
#pragma unroll
    for (int j = 0; j < 8; ++j) mine[j * 64] = acc[j][1];
    bar();
    LAS f32x4* other = (LAS f32x4*)(smem + SCR + (wave ^ 1) * 8192) + lane;
#pragma unroll
    for (int j = 0; j < 8; ++j) acc[j][0] += other[j * 64];
    bar();
    LAS char* qimg = smem + SCR + (wave >> 1) * 8192;
    const int cc = lane & 15, g = lane >> 4, q = cc + 16 * c.kh;
#pragma unroll
    for (int e = 0; e < 4; ++e)
      *(LAS u32x4*)(qimg + q * 256 + 16 * ((4 * g + e) ^ cc)) = u32x4{cvtpk(acc[0][0][e], acc[1][0][e]), cvtpk(acc[2][0][e], acc[3][0][e]), cvtpk(acc[4][0][e], acc[5][0][e]), cvtpk(acc[6][0][e], acc[7][0][e])};
    bar();
#pragma unroll
    for (int s = 0; s < 8; ++s) qf[s] = *(LAS bf16x8*)(qimg + (lane & 31) * 256 + 16 * ((2 * s + (lane >> 5)) ^ (lane & 15)));
  }
  const unsigned long long tag64 = *(const __attribute__((address_space(4))) unsigned long long*)(ws + WS_GEN) + 1ull;
  const unsigned tag = (unsigned)tag64;
  TAKE(2);

  f32x16 o[4] = {};
  float m = -__builtin_inff(), l = 0.f;
  if (br) attn_loop<NS_B, NT_B>(c, qf, o, m, l); else attn_loop<NS_A, NT_A>(c, qf, o, m, l);
  SBAR();
  DMA_W(0, WPRE);
  TAKE(3);

  GAS unsigned long long* gen = (GAS unsigned long long*)(ws + WS_GEN);
  GAS unsigned* flags = (GAS unsigned*)(ws + WS_FLAG);

  const int r32 = lane & 31, hi = lane >> 5;
  { auto rr = __builtin_amdgcn_permlane32_swap(__float_as_uint(l), __float_as_uint(l), false, false); l = __uint_as_float(rr[0]) + __uint_as_float(rr[1]); }
  {
    LAS f32x4* mine = (LAS f32x4*)(smem + SCR + wave * 8192) + lane;
    LAS f32x4* other = (LAS f32x4*)(smem + SCR + (wave ^ 1) * 8192) + lane;
    if (hi == 0) *(LAS f32x2*)(smem + c.ring + 15360 + r32 * 8) = f32x2{m, l};
#pragma unroll
    for (int u = 0; u < 8; ++u) mine[u * 64] = f32x4{o[2 + (u >> 2)][4 * (u & 3)], o[2 + (u >> 2)][4 * (u & 3) + 1], o[2 + (u >> 2)][4 * (u & 3) + 2], o[2 + (u >> 2)][4 * (u & 3) + 3]};
    ESTAMP(8);
    bar();
    ESTAMP(9);
    const f32x2 v = *(LAS f32x2*)(smem + (c.ring ^ 32768u) + 15360 + r32 * 8);
    const float ms = fmaxf(m, v[0]), e0 = __builtin_amdgcn_exp2f((m - ms) * SC2), e1 = __builtin_amdgcn_exp2f((v[0] - ms) * SC2);
    const float inv = 1.f / fmaf(e0, l, e1 * v[1]), f0 = e0 * inv, f1 = e1 * inv;
    const rsrc_t srdOw = mk_rsrc(ws + WS_O + (size_t)((br * NB + b) * NH + head) * 8192, 8192);
#pragma unroll
    for (int u = 0; u < 4; ++u) {
      const f32x4 a0 = other[(2 * u) * 64] * f1, a1 = other[(2 * u + 1) * 64] * f1;
      const int cb = u >> 1, r0 = 8 * (u & 1);
      const u32x4 w = {cvtpk(fmaf(o[cb][r0], f0, a0[0]), fmaf(o[cb][r0 + 1], f0, a0[1])), cvtpk(fmaf(o[cb][r0 + 2], f0, a0[2]), fmaf(o[cb][r0 + 3], f0, a0[3])),
                       cvtpk(fmaf(o[cb][r0 + 4], f0, a1[0]), fmaf(o[cb][r0 + 5], f0, a1[1])), cvtpk(fmaf(o[cb][r0 + 6], f0, a1[2]), fmaf(o[cb][r0 + 7], f0, a1[3]))};
      __builtin_amdgcn_raw_buffer_store_b128(w, srdOw, lane * 16, (c.kh * 4 + u) * 1024, 16);
    }
  }
  asm volatile("s_waitcnt lgkmcnt(0)" ::: "memory");
  ESTAMP(10);
  DMA_W(WPRE, WPRE + WMID);
  ESTAMP(11);
  vmwait<WMID>();
  ESTAMP(12);
  bar();
  if (threadIdx.x < 2) __hip_atomic_store(flags + (threadIdx.x * NB + b) * NH + head, tag, __ATOMIC_RELAXED, __HIP_MEMORY_SCOPE_AGENT);
  DMA_W(WPRE + WMID, 32);
  TAKE(4);

  {
    GAS unsigned* fp = flags + (obr * NB + ob + ((lane >> 1) & 1)) * NH + h0 + (lane & 1);
    for (unsigned spins = 0; spins < SPIN_CAP; ++spins) {
      if (__all(__hip_atomic_load(fp, __ATOMIC_RELAXED, __HIP_MEMORY_SCOPE_AGENT) == tag)) break;
      __builtin_amdgcn_s_sleep(FSLEEP);
    }
    vmwait<0>();
  }
  const int blk = (lane >> 4) & 1, q4 = (lane & 15) >> 2, p4 = lane & 3;
  const unsigned waddr = c.ring + 128 * (8 * hi + q4) + 64 * (q4 >> 1) + 32 * blk + 8 * p4;
  bf16x8 wf[16][2];
#define RD_W(S0, S1) _Pragma("unroll") for (int s = (S0); s < (S1); ++s) _Pragma("unroll") for (int nb = 0; nb < 2; ++nb) { \
    const bf16x4 lo = __builtin_amdgcn_ds_read_tr16_b64_v4bf16((LAS bf16x4*)(smem + (waddr ^ (64u * nb)) + 2048u * ((s + 8) & 15))); \
    const bf16x4 up = __builtin_amdgcn_ds_read_tr16_b64_v4bf16((LAS bf16x4*)(smem + (waddr ^ (64u * nb)) + 2048u * ((s + 8) & 15) + 512u)); \
    wf[s][nb] = __builtin_shufflevector(lo, up, 0, 1, 2, 3, 4, 5, 6, 7); }
  TAKE(5);
  const rsrc_t srdO = mk_rsrc(ws + WS_O + (size_t)((obr * NB + ob) * NH + h0) * 8192, NH * 8192 + 2 * 8192);
  u32x4 of[16][2];
#define O_ISSUE(S0, S1) _Pragma("unroll") for (int s = (S0); s < (S1); ++s) _Pragma("unroll") for (int bi = 0; bi < 2; ++bi) \
    of[s][bi] = bload_sc1(srdO, lane * 16, (unsigned)((s + 2 * c.orot) & 15) * 1024u + bi * (NH * 8192u))
#define O_WAIT(S, N) asm volatile("s_waitcnt vmcnt(%4)" : "+v"(of[S][0]), "+v"(of[S][1]), "+v"(of[(S) + 1][0]), "+v"(of[(S) + 1][1]) : "n"(N) : "memory")
  O_ISSUE(0, 12);
  RD_W(0, 4);
  f32x16 acc[4] = {};
#pragma unroll
  for (int s0 = 0; s0 < 16; s0 += 2) {
    SBAR();
    O_WAIT(s0, s0 < 4 ? 20 : 28 - 2 * s0);
    if (s0 < 4) O_ISSUE(12 + s0, 14 + s0);
    if (s0 + 4 < 16) RD_W(s0 + 4, s0 + 6);
    SBAR();
#pragma unroll
    for (int s = s0; s < s0 + 2; ++s)
#pragma unroll
      for (int j = 0; j < 4; ++j) acc[j] = __builtin_amdgcn_mfma_f32_32x32x16_bf16(wf[s][j & 1], __builtin_bit_cast(bf16x8, of[s][j >> 1]), acc[j], 0, 0, 0);
  }
  TAKE(6);
#pragma unroll
  for (int u = 0; u < 16; ++u) *((LAS f32x4*)(smem + c.ring) + u * 64 + lane) = f32x4{acc[u >> 2][4 * (u & 3)], acc[u >> 2][4 * (u & 3) + 1], acc[u >> 2][4 * (u & 3) + 2], acc[u >> 2][4 * (u & 3) + 3]};
  bar();
  {
    const unsigned thi = (unsigned)(tag64 >> 12), tlo = tag << 20;
    f32x4 a;
#pragma unroll
    for (int r = 3; r >= 0; --r) {
      const LAS f32x4* red = (const LAS f32x4*)smem + ((okq ^ r) * 4 + wave) * 64 + lane;
      a = (red[0] + red[2048]) + (red[4096] + red[6144]);
      if (r && !NOBOX) {
        const rsrc_t srdB = mk_rsrc(ws + WS_BOX + (size_t)(blockIdx.x ^ r) * 32768, 32768);
#pragma unroll
        for (int e = 0; e < 2; ++e) {
          const unsigned u0 = __float_as_uint(a[2 * e]), u1 = __float_as_uint(a[2 * e + 1]);
          const u32x4 w = {tlo | ((u0 + 0x7ffu + ((u0 >> 12) & 1u)) >> 12), thi, tlo | ((u1 + 0x7ffu + ((u1 >> 12) & 1u)) >> 12), thi};
          __builtin_amdgcn_raw_buffer_store_b128(w, srdB, threadIdx.x * 16, (r * 2 + e) * 4096, 16);
        }
      }
    }
    const rsrc_t srdI = mk_rsrc(ws + WS_BOX + (size_t)blockIdx.x * 32768, 32768);
    u32x4 g[6] = {};
#define G_OK(i) ((g[i][1] == thi) & (g[i][3] == thi) & ((((g[i][0] ^ tlo) | (g[i][2] ^ tlo)) >> 20) == 0))
    for (unsigned spins = 0; spins < (NOBOX ? 0 : SPIN_CAP); ++spins) {
      bool ok = true;
#pragma unroll
      for (int i = 0; i < 6; ++i) g[i] = __builtin_amdgcn_raw_buffer_load_b128(srdI, threadIdx.x * 16, (2 + i) * 4096, 16);
#pragma unroll
      for (int i = 0; i < 6; ++i) ok &= G_OK(i);
      if (__all(ok)) break;
      asm volatile("s_sleep %0" :: "n"(PSLEEP) : "memory");
    }
#pragma unroll
    for (int i = 0; i < 6; ++i) { a[2 * (i & 1)] += __uint_as_float(g[i][0] << 12); a[2 * (i & 1) + 1] += __uint_as_float(g[i][2] << 12); }
    *(u32x2*)(out + (size_t)((ob + (okq >> 1)) * SQ + r32) * 1024 + obr * 512 + ocb * 64 + (okq & 1) * 32 + 8 * wave + 4 * hi) = u32x2{cvtpk(a[0], a[1]), cvtpk(a[2], a[3])};
  }
  if (blockIdx.x == 0 && wave == 0) {
    for (unsigned spins = 0; spins < SPIN_CAP; ++spins) {
      bool ok = true;
#pragma unroll
      for (int i = 0; i < 8; ++i) ok &= __hip_atomic_load(flags + i * 64 + lane, __ATOMIC_RELAXED, __HIP_MEMORY_SCOPE_AGENT) == tag;
      if (__all(ok)) break;
      __builtin_amdgcn_s_sleep(1);
    }
    if (lane == 0) __hip_atomic_store(gen, tag64, __ATOMIC_RELAXED, __HIP_MEMORY_SCOPE_AGENT);
  }
  TAKE(7);
}

extern "C" void kernel_launch(void* const* d_in, const int* in_sizes, int n_in, void* d_out, int out_size, void* d_ws, size_t ws_size, hipStream_t stream) {
  (void)in_sizes; (void)n_in; (void)out_size; (void)ws_size;
  (void)hipFuncSetAttribute((const void*)fused_kernel, hipFuncAttributeMaxDynamicSharedMemorySize, LDS1);
  hipLaunchKernelGGL(fused_kernel, dim3(256), dim3(256), LDS1, stream, d_in[0], d_in[1], d_in[2], d_in[3], d_in[4], d_in[5], d_in[6], d_in[7], d_in[8], d_in[9],
                     (char*)d_ws, (__hip_bfloat16*)d_out);
}
```

```cpp
#include <hip/hip_runtime.h>
#include <hip/hip_bf16.h>
#include <stdint.h>

#define DI __device__ __forceinline__
#define LAS __attribute__((address_space(3)))
#define GAS __attribute__((address_space(1)))

typedef __attribute__((ext_vector_type(8))) __bf16 bf16x8;
typedef __attribute__((ext_vector_type(4))) __bf16 bf16x4;
typedef __attribute__((ext_vector_type(2))) __bf16 bf16x2;
typedef __attribute__((ext_vector_type(2))) float f32x2;
typedef __attribute__((ext_vector_type(4))) float f32x4;
typedef __attribute__((ext_vector_type(16))) float f32x16;
typedef __attribute__((ext_vector_type(4))) unsigned u32x4;
typedef __attribute__((ext_vector_type(2))) unsigned u32x2;
typedef __amdgpu_buffer_rsrc_t rsrc_t;

constexpr int NB = 8, SQ = 32, DM = 512, NH = 32, SK = 2048, DH = 128;
constexpr int NT = 32;
constexpr unsigned SCR = 131072;
constexpr unsigned LDS1 = 163840;
constexpr float SC2 = 0.08838834764831845f * 1.4426950408889634f;
constexpr unsigned WS_GEN = 0, WS_FLAG = 4096, WS_O = 262144, WS_BOX = WS_O + (4u << 20);
constexpr unsigned SPIN_CAP = 1u << 17;

#define A_OLD 1
#define NS_A 2
#define NS_B 2
#define NT_A 0
#define NT_B 1
#define OMAP 0
#define FTWAIT 0
#define QSCHED 1
#define PSLEEP 1
#define FSLEEP 4
#define NOBOX 0
#define PRECLR 0
#define OROT ((ob + ocb) & 7)
#define WPRE 0
#define WMID 0
#define WROT (ocb + (ob >> 1))
#define BOXPOLL 1
#define WL 0
#define WT0 8
#define EARLYPOLL 0
#define FS 32
#define OUTAUX 16
#define LATE23 0
#define SMPOS 2
#define KPOS 5
#define VPOS 7
#define TROT 0
#define PRETOUCH 0
#define QBUF 3
#define TAKE(k)

DI unsigned rfl(unsigned x) { return __builtin_amdgcn_readfirstlane(x); }
DI rsrc_t mk_rsrc(const void* p, unsigned bytes) { return __builtin_amdgcn_make_buffer_rsrc((void*)p, 0, bytes, 0x00020000); }
DI unsigned cvtpk(float lo, float hi) { f32x2 v = {lo, hi}; bf16x2 b = __builtin_convertvector(v, bf16x2); return __builtin_bit_cast(unsigned, b); }
DI void bar() { asm volatile("s_waitcnt lgkmcnt(0)\n\ts_barrier" ::: "memory"); }
template <int N> DI void vmwait() { asm volatile("s_waitcnt vmcnt(%0)" :: "n"(N) : "memory"); }
#define ESTAMP(k)
#define CONTIG 0
#define TOFF(t) (((((t) + c.tile0) & (NT - 1)) * 2 + c.kh) * 8192u)
#define VSHIFT 0
#define SBAR() __builtin_amdgcn_sched_barrier(0)
#define SEGT(i)

#define DMA_TILE(NTS) asm volatile( \
    "s_waitcnt lgkmcnt(0)\n\t" \
    "s_mov_b32 m0, %0\n\ts_nop 0\n\t" \
    "buffer_load_dwordx4 %5, %4, %2 offen" NTS " lds\n\t" \
    "buffer_load_dwordx4 %6, %4, %2 offen offset:1024" NTS " lds\n\t" \
    "buffer_load_dwordx4 %7, %4, %2 offen offset:2048" NTS " lds\n\t" \
    "buffer_load_dwordx4 %8, %4, %2 offen offset:3072" NTS " lds\n\t" \
    "s_mov_b32 m0, %1\n\ts_nop 0\n\t" \
    "buffer_load_dwordx4 %5, %4, %3 offen" NTS " lds\n\t" \
    "buffer_load_dwordx4 %6, %4, %3 offen offset:1024" NTS " lds\n\t" \
    "buffer_load_dwordx4 %7, %4, %3 offen offset:2048" NTS " lds\n\t" \
    "buffer_load_dwordx4 %8, %4, %3 offen offset:3072" NTS " lds" \
    :: "s"(lds), "s"(lds + 4096u), "s"(soff), "s"(soff + 4096u), "s"(srd), "v"(vo[0]), "v"(vo[1]), "v"(vo[2]), "v"(vo[3]) : "memory")
DI void dma_tile(bool nt, unsigned lds, rsrc_t srd, unsigned soff, const unsigned (&vo)[4]) {
  if (nt) DMA_TILE(" nt"); else DMA_TILE("");
}
DI u32x4 bload(rsrc_t srd, unsigned voff, unsigned soff) {
  u32x4 r; asm volatile("buffer_load_dwordx4 %0, %1, %2, %3 offen" : "=v"(r) : "v"(voff), "s"(srd), "s"(soff) : "memory"); return r;
}
DI u32x4 bload_sc1(rsrc_t srd, unsigned voff, unsigned soff) {
  u32x4 r; asm volatile("buffer_load_dwordx4 %0, %1, %2, %3 offen sc1" : "=v"(r) : "v"(voff), "s"(srd), "s"(soff) : "memory"); return r;
}

struct Ctx { LAS char* smem; unsigned lds0, ring; rsrc_t srdK, srdV, srdWo; unsigned vo[4], wvo, wso, tile0; int lane, kh, orot; bool nt;
};

template <int NS> DI void qproj(const Ctx& c, rsrc_t srdX, rsrc_t srdW, int head, int xrot, int wrot, f32x4 (&acc)[8][2]) {
  const int cc = c.lane & 15, g = c.lane >> 4;
  u32x4 wr[8][8];
  const unsigned wvo = g * 65536 + 16 * cc;
  const unsigned xvo = (c.lane >> 5) * 1024 + 16 * ((c.lane & 31) ^ (c.lane >> 5));
  const unsigned ximg = c.ring + 16384;
#define XISSUE(I0, I1) _Pragma("unroll") for (int i = (I0); i < (I1); ++i) { const unsigned p = (unsigned)(i + 4 * xrot) & 15u; \
    asm volatile("s_mov_b32 m0, %0\n\ts_nop 0\n\tbuffer_load_dwordx4 %1, %2, %3 offen lds" :: "s"(c.lds0 + ximg + 1024u * p), "v"(xvo ^ (32u * (p & 7u))), "s"(srdX), "s"(p * 2048u + c.kh * 512u) : "memory"); }
#define QISSUE(ks) do { const unsigned kk = (unsigned)(c.kh * 8 + (((ks) + wrot) & 7)); \
    _Pragma("unroll") for (int i = 0; i < 8; ++i) wr[ks][i] = bload(srdW, wvo, (kk * 32 + i) * 8192 + head * 256); } while (0)
  static_assert((KPOS < 0 || KPOS >= 7 - QBUF) && (VPOS < 0 || VPOS >= 7 - QBUF) && KPOS <= VPOS, "positions");
  XISSUE(0, 4); QISSUE(0);
  if (KPOS < 0) dma_tile(c.nt, c.lds0 + c.ring, c.srdK, TOFF(0), c.vo);
  if (VPOS < 0) dma_tile(c.nt, c.lds0 + c.ring + 8192u, c.srdV, TOFF(0), c.vo);
  XISSUE(4, 16);
#pragma unroll
  for (int ks = 1; ks < QBUF; ++ks) QISSUE(ks);
#pragma unroll
  for (int ks = 0; ks < 8; ++ks) {
    SBAR();
    const int nq = (ks + QBUF - 1 < 7 ? ks + QBUF - 1 : 7) - ks;
    const int n = nq * 8 + (KPOS >= 4 && ks > KPOS ? 8 : 0) + (VPOS >= 4 && ks > VPOS ? 8 : 0);
    asm volatile("s_waitcnt vmcnt(%8)" : "+v"(wr[ks][0]), "+v"(wr[ks][1]), "+v"(wr[ks][2]), "+v"(wr[ks][3]), "+v"(wr[ks][4]), "+v"(wr[ks][5]), "+v"(wr[ks][6]), "+v"(wr[ks][7]) : "n"(n) : "memory");
    if (ks + QBUF < 8) QISSUE((ks + QBUF) & 7);
    SBAR();
    bf16x8 xf[2];
    const unsigned ch = 4u * ((ks + wrot) & 7) + g;
#pragma unroll
    for (int qb = 0; qb < 2; ++qb) xf[qb] = *(LAS bf16x8*)(c.smem + ximg + (cc + 16 * (qb ^ c.kh)) * 512 + 16 * (ch ^ cc));
    if (NS == 4 && ks == 7 && !LATE23) {
      if (VPOS == 7) dma_tile(c.nt, c.lds0 + c.ring + 8192u, c.srdV, TOFF(0), c.vo);
      dma_tile(c.nt, c.lds0 + c.ring + 16384u, c.srdK, TOFF(1), c.vo);
      dma_tile(c.nt, c.lds0 + c.ring + 24576u, c.srdV, TOFF(1), c.vo);
      SBAR();
    }
#pragma unroll
    for (int j = 0; j < 8; ++j) {
      u32x4 f;
#pragma unroll
      for (int m = 0; m < 4; ++m) f[m] = __builtin_amdgcn_perm(wr[ks][2 * m + 1][j >> 1], wr[ks][2 * m][j >> 1], (j & 1) ? 0x07060302u : 0x05040100u);
#pragma unroll
      for (int qb = 0; qb < 2; ++qb)
        acc[j][qb] = __builtin_amdgcn_mfma_f32_16x16x32_bf16(__builtin_bit_cast(bf16x8, f), xf[qb], ks ? acc[j][qb] : f32x4{0.f, 0.f, 0.f, 0.f}, 0, 0, 0);
    }
    __builtin_amdgcn_sched_group_barrier(0x100, 2, 0);
    __builtin_amdgcn_sched_group_barrier(0x2, 8, 0);
#pragma unroll
    for (int j = 0; j < 12; ++j) { __builtin_amdgcn_sched_group_barrier(0x8, 1, 0); __builtin_amdgcn_sched_group_barrier(0x2, 2, 0); }
    __builtin_amdgcn_sched_group_barrier(0x8, 4, 0);
    SBAR();
    if (ks == KPOS) dma_tile(c.nt, c.lds0 + c.ring, c.srdK, TOFF(0), c.vo);
    if (ks == VPOS && !(NS == 4 && ks == 7 && !LATE23)) dma_tile(c.nt, c.lds0 + c.ring + 8192u, c.srdV, TOFF(0), c.vo);
    if (NS == 4 && ks == 7 && LATE23) {
      dma_tile(c.nt, c.lds0 + c.ring + 16384u, c.srdK, TOFF(1), c.vo);
      if (LATE23 == 1) dma_tile(c.nt, c.lds0 + c.ring + 24576u, c.srdV, TOFF(1), c.vo);
    }
  }
#undef XISSUE
#undef QISSUE
}

#define DMA_W(I0, I1) _Pragma("unroll") for (int i = (I0); i < (I1); ++i) \
    asm volatile("s_mov_b32 m0, %0\n\ts_nop 0\n\tbuffer_load_dwordx4 %1, %2, %3 offen lds" :: "s"(c.lds0 + c.ring + 1024u * ((i + 16) & 31)), "v"(c.wvo), "s"(c.srdWo), \
                 "s"((unsigned)((((i >> 2) + c.orot) & 7) * 32 + (i & 3) * 8) * 1024u + c.wso) : "memory")

template <int NS> DI void attn_loop(const Ctx& c, const bf16x8 (&qf)[8], f32x16 (&o)[4], float& m, float& l) {
  const int lane = c.lane, r32 = lane & 31, hi = lane >> 5;
  const unsigned kaddr = c.ring + 256 * r32 + 16 * (hi ^ (((r32 & 3) << 2) | ((r32 >> 2) & 3)));
  const int blk = (lane >> 4) & 1, q4 = (lane & 15) >> 2, p4 = lane & 3;
  const unsigned vaddr = c.ring + 8192 + 256 * (4 * hi + q4) + 64 * q4 + 16 * ((2 * blk + (p4 & 1)) ^ hi) + 8 * (p4 >> 1);
  const unsigned vx = c.kh * 128u;
  static_assert(NS == 2 || NS == 4, "ring");
  constexpr int LA = NS / 2;
#pragma unroll 1
  for (int t0 = 0; t0 < NT; t0 += LA) {
#pragma unroll
    for (int u = 0; u < LA; ++u) {
      const int t = t0 + u;
      const unsigned so = u * 16384u;
      const bool more = t + LA < NT;
      const unsigned soffn = TOFF(t + LA);
      SEGT(0);
      if (more || u == 0) vmwait<(NS - 1) * 8>(); else vmwait<8>();
      SEGT(1);
      bf16x8 kf[8];
#pragma unroll
      for (int s = 0; s < 8; ++s) kf[s] = *(LAS bf16x8*)(c.smem + (kaddr ^ (32u * s)) + so);
      SEGT(2);
      if (more) dma_tile(c.nt, c.lds0 + c.ring + so, c.srdK, soffn, c.vo);
      SEGT(3);
      f32x16 sc = {};
#pragma unroll
      for (int s = 0; s < 8; ++s) sc = __builtin_amdgcn_mfma_f32_32x32x16_bf16(kf[s], qf[s], sc, 0, 0, 0);
      float mx = sc[0];
#pragma unroll
      for (int i = 1; i < 16; ++i) mx = fmaxf(mx, sc[i]);
      { auto rr = __builtin_amdgcn_permlane32_swap(__float_as_uint(mx), __float_as_uint(mx), false, false); mx = fmaxf(__uint_as_float(rr[0]), __uint_as_float(rr[1])); }
      const float mn = fmaxf(m, mx), alpha = __builtin_amdgcn_exp2f((m - mn) * SC2), nm = -mn * SC2;
      m = mn;
      float ps = 0.f;
#pragma unroll
      for (int i = 0; i < 16; ++i) { sc[i] = __builtin_amdgcn_exp2f(fmaf(sc[i], SC2, nm)); ps += sc[i]; }
      l = fmaf(l, alpha, ps);
      bf16x8 pf[2];
#pragma unroll
      for (int s = 0; s < 2; ++s) pf[s] = __builtin_bit_cast(bf16x8, u32x4{cvtpk(sc[8 * s], sc[8 * s + 1]), cvtpk(sc[8 * s + 2], sc[8 * s + 3]), cvtpk(sc[8 * s + 4], sc[8 * s + 5]), cvtpk(sc[8 * s + 6], sc[8 * s + 7])});
      asm volatile("" : "+v"(pf[0]), "+v"(pf[1]), "+v"(l));
#pragma unroll
      for (int cb = 0; cb < 4; ++cb) o[cb] *= alpha;
      asm volatile("" : "+v"(o[0]), "+v"(o[1]), "+v"(o[2]), "+v"(o[3]));
      SEGT(4);
      if (more) vmwait<(NS - 1) * 8>(); else if (u == 0) vmwait<(NS - 2) * 8>(); else vmwait<0>();
      SEGT(5);
      bf16x8 vf[4][2];
#pragma unroll
      for (int cb = 0; cb < 4; ++cb)
#pragma unroll
        for (int s = 0; s < 2; ++s) {
          const bf16x4 lo = __builtin_amdgcn_ds_read_tr16_b64_v4bf16((LAS bf16x4*)(c.smem + ((vaddr ^ vx ^ (64u * cb)) + 4096u * s + so)));
          const bf16x4 up = __builtin_amdgcn_ds_read_tr16_b64_v4bf16((LAS bf16x4*)(c.smem + ((vaddr ^ vx ^ (64u * cb) ^ 32u) + 4096u * s + 2048u + so)));
          vf[cb][s] = __builtin_shufflevector(lo, up, 0, 1, 2, 3, 4, 5, 6, 7);
        }
      SEGT(6);
      if (more) dma_tile(c.nt, c.lds0 + c.ring + 8192u + so, c.srdV, soffn, c.vo);
      SEGT(7);
#pragma unroll
      for (int s = 0; s < 2; ++s)
#pragma unroll
        for (int cb = 0; cb < 4; ++cb) o[cb] = __builtin_amdgcn_mfma_f32_32x32x16_bf16(vf[cb][s], pf[s], o[cb], 0, 0, 0);
    }
  }
}

__global__ __launch_bounds__(256, 2) void fused_kernel(const void* __restrict__ xA, const void* __restrict__ kA, const void* __restrict__ vA, const void* __restrict__ wqA, const void* __restrict__ woA,
                                                       const void* __restrict__ xB, const void* __restrict__ kB, const void* __restrict__ vB, const void* __restrict__ wqB, const void* __restrict__ woB,
                                                       char* ws, __hip_bfloat16* __restrict__ out) {
  extern __shared__ __attribute__((aligned(16))) char smem_[];
  Ctx c;
  c.smem = (LAS char*)smem_; c.lds0 = (unsigned)(uintptr_t)c.smem;
  LAS char* smem = c.smem;
  const int lane = threadIdx.x & 63;
  const int wave = rfl(threadIdx.x >> 6), br = (wave >> 1) ^ (A_OLD ^ 1);
  TAKE(0);
  c.lane = lane; c.kh = wave & 1; c.nt = br ? NT_B : NT_A; c.ring = wave * 32768u;
  const int g8 = blockIdx.x & 7, jj = blockIdx.x >> 3, b = jj >> 2, head = 4 * g8 + (jj & 3);
  const size_t kvoff = (size_t)(br ? b * 32 + head : b * 8 + g8) * (SK * DH * 2);
  c.srdK = mk_rsrc((const char*)(br ? kB : kA) + kvoff, SK * DH * 2);
  c.srdV = mk_rsrc((const char*)(br ? vB : vA) + kvoff, SK * DH * 2);
  const rsrc_t srdW = mk_rsrc(br ? wqB : wqA, DM * NH * DH * 2);
  const rsrc_t srdX = mk_rsrc((const char*)(br ? xB : xA) + (size_t)b * (SQ * DM * 2), SQ * DM * 2);
#pragma unroll
  for (int i = 0; i < 4; ++i) c.vo[i] = (lane >> 4) * 256 + 16 * ((lane & 15) ^ (((lane >> 4) << 2) | i));

  c.tile0 = TROT == 0 ? 0u : TROT == 1 ? (unsigned)((br ? blockIdx.x * 11 : (b * 8 + g8) * 5 + 2) & 31) : (unsigned)(((br ? blockIdx.x : b * 8 + g8 + 300) * 2654435761u) >> 27);
  const int obr = g8 >> 2, okq = g8 & 3, ob = 2 * (jj >> 3), ocb = jj & 7, h0 = okq * 8 + ((wave + WROT) & 3) * 2;
  c.orot = OROT;
  c.srdWo = mk_rsrc(obr ? woB : woA, NH * DH * DM * 2);
  c.wvo = (lane >> 3) * 1024 + 16 * ((lane & 7) ^ (((lane >> 4) & 1) << 2));
  c.wso = h0 * 128 * 1024 + ocb * 128;
  bf16x8 qf[8];
  {
    f32x4 acc[8][2];
    qproj<NS_A>(c, srdX, srdW, head, jj & 3, b, acc);
    TAKE(1);
    LAS f32x4* mine = (LAS f32x4*)(smem + SCR + wave * 8192) + lane;
#pragma unroll
    for (int j = 0; j < 8; ++j) mine[j * 64] = acc[j][1];
    if (LATE23 == 2 && NS_A == 4) dma_tile(c.nt, c.lds0 + c.ring + 24576u, c.srdV, TOFF(1), c.vo);
    bar();
    LAS f32x4* other = (LAS f32x4*)(smem + SCR + (wave ^ 1) * 8192) + lane;
#pragma unroll
    for (int j = 0; j < 8; ++j) acc[j][0] += other[j * 64];
    bar();
    LAS char* qimg = smem + SCR + (wave >> 1) * 8192;
    const int cc = lane & 15, g = lane >> 4, q = cc + 16 * c.kh;
#pragma unroll
    for (int e = 0; e < 4; ++e)
      *(LAS u32x4*)(qimg + q * 256 + 16 * ((4 * g + e) ^ cc)) = u32x4{cvtpk(acc[0][0][e], acc[1][0][e]), cvtpk(acc[2][0][e], acc[3][0][e]), cvtpk(acc[4][0][e], acc[5][0][e]), cvtpk(acc[6][0][e], acc[7][0][e])};
    bar();
#pragma unroll
    for (int s = 0; s < 8; ++s) qf[s] = *(LAS bf16x8*)(qimg + (lane & 31) * 256 + 16 * ((2 * s + (lane >> 5)) ^ (lane & 15)));
  }
  const unsigned long long tag64 = *(const __attribute__((address_space(4))) unsigned long long*)(ws + WS_GEN) + 1ull;
  const unsigned tag = (unsigned)tag64;
  TAKE(2);

  f32x16 o[4] = {};
  float m = -__builtin_inff(), l = 0.f;
  attn_loop<NS_A>(c, qf, o, m, l);
  TAKE(3);

  GAS unsigned long long* gen = (GAS unsigned long long*)(ws + WS_GEN);
  GAS unsigned* flags = (GAS unsigned*)(ws + WS_FLAG);

  const int r32 = lane & 31, hi = lane >> 5;
  { auto rr = __builtin_amdgcn_permlane32_swap(__float_as_uint(l), __float_as_uint(l), false, false); l = __uint_as_float(rr[0]) + __uint_as_float(rr[1]); }
  {
    LAS f32x4* mine = (LAS f32x4*)(smem + SCR + wave * 8192) + lane;
    LAS f32x4* other = (LAS f32x4*)(smem + SCR + (wave ^ 1) * 8192) + lane;
    if (hi == 0) *(LAS f32x2*)(smem + c.ring + 15360 + r32 * 8) = f32x2{m, l};
#pragma unroll
    for (int u = 0; u < 8; ++u) mine[u * 64] = f32x4{o[2 + (u >> 2)][4 * (u & 3)], o[2 + (u >> 2)][4 * (u & 3) + 1], o[2 + (u >> 2)][4 * (u & 3) + 2], o[2 + (u >> 2)][4 * (u & 3) + 3]};
    ESTAMP(8);
    bar();
    ESTAMP(9);
    const f32x2 v = *(LAS f32x2*)(smem + (c.ring ^ 32768u) + 15360 + r32 * 8);
    const float ms = fmaxf(m, v[0]), e0 = __builtin_amdgcn_exp2f((m - ms) * SC2), e1 = __builtin_amdgcn_exp2f((v[0] - ms) * SC2);
    const float inv = 1.f / fmaf(e0, l, e1 * v[1]), f0 = e0 * inv, f1 = e1 * inv;
    const rsrc_t srdOw = mk_rsrc(ws + WS_O + (size_t)((br * NB + b) * NH + head) * 8192, 8192);
#pragma unroll
    for (int u = 0; u < 4; ++u) {
      const f32x4 a0 = other[(2 * u) * 64] * f1, a1 = other[(2 * u + 1) * 64] * f1;
      const int cb = u >> 1, r0 = 8 * (u & 1);
      const u32x4 w = {cvtpk(fmaf(o[cb][r0], f0, a0[0]), fmaf(o[cb][r0 + 1], f0, a0[1])), cvtpk(fmaf(o[cb][r0 + 2], f0, a0[2]), fmaf(o[cb][r0 + 3], f0, a0[3])),
                       cvtpk(fmaf(o[cb][r0 + 4], f0, a1[0]), fmaf(o[cb][r0 + 5], f0, a1[1])), cvtpk(fmaf(o[cb][r0 + 6], f0, a1[2]), fmaf(o[cb][r0 + 7], f0, a1[3]))};
      __builtin_amdgcn_raw_buffer_store_b128(w, srdOw, lane * 16, (c.kh * 4 + u) * 1024, 16);
    }
  }
  asm volatile("s_waitcnt lgkmcnt(0)" ::: "memory");
  ESTAMP(10);
  ESTAMP(11);
  vmwait<0>();
  ESTAMP(12);
  bar();
  if (threadIdx.x < 2) __hip_atomic_store(flags + ((threadIdx.x * NB + b) * NH + head) * FS, tag, __ATOMIC_RELAXED, __HIP_MEMORY_SCOPE_AGENT);
  const int blk = (lane >> 4) & 1, q4 = (lane & 15) >> 2, p4 = lane & 3;
  const unsigned waddr = c.ring + 128 * (8 * hi + q4) + 64 * (q4 >> 1) + 32 * blk + 8 * p4;
  const rsrc_t srdO = mk_rsrc(ws + WS_O + (size_t)((obr * NB + ob) * NH + h0) * 8192, NH * 8192 + 2 * 8192);
  GAS unsigned* fp = flags + ((obr * NB + ob + ((lane >> 1) & 1)) * NH + h0 + (lane & 1)) * FS;
  f32x16 acc[4] = {};
#define RD_W(S0, S1) _Pragma("unroll") for (int s = (S0); s < (S1); ++s) _Pragma("unroll") for (int nb = 0; nb < 2; ++nb) { \
    const bf16x4 lo = __builtin_amdgcn_ds_read_tr16_b64_v4bf16((LAS bf16x4*)(smem + (waddr ^ (64u * nb)) + 2048u * ((s + 8) & 15))); \
    const bf16x4 up = __builtin_amdgcn_ds_read_tr16_b64_v4bf16((LAS bf16x4*)(smem + (waddr ^ (64u * nb)) + 2048u * ((s + 8) & 15) + 512u)); \
    wf[s][nb] = __builtin_shufflevector(lo, up, 0, 1, 2, 3, 4, 5, 6, 7); }
#define O_ISSUE(S0, S1) _Pragma("unroll") for (int s = (S0); s < (S1); ++s) _Pragma("unroll") for (int bi = 0; bi < 2; ++bi) \
    of[s][bi] = bload_sc1(srdO, lane * 16, (unsigned)((s + 2 * c.orot) & 15) * 1024u + bi * (NH * 8192u))
#define O_WAIT(S, N) asm volatile("s_waitcnt vmcnt(%4)" : "+v"(of[S][0]), "+v"(of[S][1]), "+v"(of[(S) + 1][0]), "+v"(of[(S) + 1][1]) : "n"(N) : "memory")
#define O_MFMA(S0) _Pragma("unroll") for (int s = (S0); s < (S0) + 2; ++s) _Pragma("unroll") for (int j = 0; j < 4; ++j) \
    acc[j] = __builtin_amdgcn_mfma_f32_32x32x16_bf16(wf[s][j & 1], __builtin_bit_cast(bf16x8, of[s][j >> 1]), acc[j], 0, 0, 0)
  bool ready = false;
  DMA_W(0, 8);
  if (EARLYPOLL == 2 && ready) {
    TAKE(4); TAKE(5);
    u32x4 of[16][2]; bf16x8 wf[16][2];
    O_ISSUE(0, 4); DMA_W(8, 16);
#pragma unroll
    for (int s0 = 0; s0 < 16; s0 += 2) {
      SBAR();
      if (s0 == 0) O_WAIT(0, 12); else if (s0 == 2) O_WAIT(2, 16); else if (s0 == 4) O_WAIT(4, 12); else if (s0 == 6) O_WAIT(6, 12);
      else if (s0 == 8) O_WAIT(8, 12); else if (s0 == 10) O_WAIT(10, 8); else if (s0 == 12) O_WAIT(12, 4); else O_WAIT(14, 0);
      RD_W(s0, s0 + 2);
      if (s0 + 4 < 16) O_ISSUE(s0 + 4, s0 + 6);
      if (s0 < 8) DMA_W(2 * s0 + 16, 2 * s0 + 20);
      SBAR();
      O_MFMA(s0);
    }
  } else {
    DMA_W(8, 32);
    TAKE(4);
    if (!ready) {
      _Pragma("unroll 1") for (unsigned spins = 0; spins < SPIN_CAP; ++spins) {
        if (__all(__hip_atomic_load(fp, __ATOMIC_RELAXED, __HIP_MEMORY_SCOPE_AGENT) == tag)) break;
        __builtin_amdgcn_s_sleep(FSLEEP);
      }
      asm volatile("" ::: "memory");
    }
    TAKE(5);
    u32x4 of[16][2]; bf16x8 wf[16][2];
    O_ISSUE(0, 12);
    vmwait<24>();
    RD_W(0, 4);
#pragma unroll
    for (int s0 = 0; s0 < 16; s0 += 2) {
      SBAR();
      O_WAIT(s0, s0 < 4 ? 20 : 28 - 2 * s0);
      if (s0 < 4) O_ISSUE(12 + s0, 14 + s0);
      if (s0 + 4 < 16) RD_W(s0 + 4, s0 + 6);
      SBAR();
      O_MFMA(s0);
    }
  }
  TAKE(6);
#pragma unroll
  for (int u = 0; u < 16; ++u) *((LAS f32x4*)(smem + c.ring) + u * 64 + lane) = f32x4{acc[u >> 2][4 * (u & 3)], acc[u >> 2][4 * (u & 3) + 1], acc[u >> 2][4 * (u & 3) + 2], acc[u >> 2][4 * (u & 3) + 3]};
  bar();
  ESTAMP(13);
  {
    const unsigned thi = (unsigned)(tag64 >> 12), tlo = tag << 20;
    f32x4 a;
#pragma unroll
    for (int r = 3; r >= 0; --r) {
      const LAS f32x4* red = (const LAS f32x4*)smem + ((okq ^ r) * 4 + wave) * 64 + lane;
      a = (red[0] + red[2048]) + (red[4096] + red[6144]);
      if (r && !NOBOX) {
        const rsrc_t srdB = mk_rsrc(ws + WS_BOX + (size_t)(blockIdx.x ^ r) * 32768, 32768);
#pragma unroll
        for (int e = 0; e < 2; ++e) {
          const unsigned u0 = __float_as_uint(a[2 * e]), u1 = __float_as_uint(a[2 * e + 1]);
          const u32x4 w = {tlo | ((u0 + 0x7ffu + ((u0 >> 12) & 1u)) >> 12), thi, tlo | ((u1 + 0x7ffu + ((u1 >> 12) & 1u)) >> 12), thi};
          __builtin_amdgcn_raw_buffer_store_b128(w, srdB, threadIdx.x * 16, (r * 2 + e) * 4096, 16);
        }
      }
    }
    ESTAMP(14);
    const rsrc_t srdI = mk_rsrc(ws + WS_BOX + (size_t)blockIdx.x * 32768, 32768);
    u32x4 g[6] = {};
#define G_OK(i) ((g[i][1] == thi) & (g[i][3] == thi) & ((((g[i][0] ^ tlo) | (g[i][2] ^ tlo)) >> 20) == 0))
    _Pragma("unroll 1") for (unsigned spins = 0; spins < (NOBOX ? 0 : SPIN_CAP); ++spins) {
      bool ok = true;
#pragma unroll
      for (int i = 0; i < 6; ++i) g[i] = __builtin_amdgcn_raw_buffer_load_b128(srdI, threadIdx.x * 16, (2 + i) * 4096, 16);
#pragma unroll
      for (int i = 0; i < 6; ++i) ok &= G_OK(i);
      if (__all(ok)) break;
      asm volatile("s_sleep %0" :: "n"(PSLEEP) : "memory");
    }
    ESTAMP(16);
#pragma unroll
    for (int i = 0; i < 6; ++i) { a[2 * (i & 1)] += __uint_as_float(g[i][0] << 12); a[2 * (i & 1) + 1] += __uint_as_float(g[i][2] << 12); }
    __builtin_amdgcn_raw_buffer_store_b64(u32x2{cvtpk(a[0], a[1]), cvtpk(a[2], a[3])}, mk_rsrc(out, NB * SQ * 2048), r32 * 2048 + 8 * hi,
                                          (ob + (okq >> 1)) * SQ * 2048 + obr * 1024 + ocb * 128 + (okq & 1) * 64 + 16 * wave, OUTAUX);
  }
  if (blockIdx.x == 0 && wave == 0) {
    _Pragma("unroll 1") for (unsigned spins = 0; spins < SPIN_CAP; ++spins) {
      bool ok = true;
#pragma unroll
      for (int i = 0; i < 8; ++i) ok &= __hip_atomic_load(flags + (i * 64 + lane) * FS, __ATOMIC_RELAXED, __HIP_MEMORY_SCOPE_AGENT) == tag;
      if (__all(ok)) break;
      __builtin_amdgcn_s_sleep(1);
    }
    if (lane == 0) __hip_atomic_store(gen, tag64, __ATOMIC_RELAXED, __HIP_MEMORY_SCOPE_AGENT);
  }
  TAKE(7);
}

extern "C" void kernel_launch(void* const* d_in, const int* in_sizes, int n_in, void* d_out, int out_size, void* d_ws, size_t ws_size, hipStream_t stream) {
  (void)in_sizes; (void)n_in; (void)out_size; (void)ws_size;
  (void)hipFuncSetAttribute((const void*)fused_kernel, hipFuncAttributeMaxDynamicSharedMemorySize, LDS1);
  hipLaunchKernelGGL(fused_kernel, dim3(256), dim3(256), LDS1, stream, d_in[0], d_in[1], d_in[2], d_in[3], d_in[4], d_in[5], d_in[6], d_in[7], d_in[8], d_in[9],
                     (char*)d_ws, (__hip_bfloat16*)d_out);
}
```

```cpp
#define DIAGK 7
#define DIAGM 0
#define STAMPS
#define PSTAMPS
#define DIAG
#include <hip/hip_runtime.h>
#include <hip/hip_bf16.h>
#include <stdint.h>

#define DI __device__ __forceinline__
#define LAS __attribute__((address_space(3)))
#define GAS __attribute__((address_space(1)))

typedef __attribute__((ext_vector_type(8))) __bf16 bf16x8;
typedef __attribute__((ext_vector_type(4))) __bf16 bf16x4;
typedef __attribute__((ext_vector_type(2))) __bf16 bf16x2;
typedef __attribute__((ext_vector_type(2))) float f32x2;
typedef __attribute__((ext_vector_type(4))) float f32x4;
typedef __attribute__((ext_vector_type(16))) float f32x16;
typedef __attribute__((ext_vector_type(4))) unsigned u32x4;
typedef __attribute__((ext_vector_type(2))) unsigned u32x2;
typedef __amdgpu_buffer_rsrc_t rsrc_t;

constexpr int NB = 8, SQ = 32, DM = 512, NH = 32, SK = 2048, DH = 128;
constexpr int NT = 32;
constexpr unsigned SCR = 131072;
constexpr unsigned LDS1 = 163840;
constexpr float SC2 = 0.08838834764831845f * 1.4426950408889634f;
constexpr unsigned WS_GEN = 0, WS_FLAG = 4096, WS_O = 262144, WS_BOX = WS_O + (4u << 20);
constexpr unsigned SPIN_CAP = 1u << 17;

#define A_OLD 1
#define NS_A 2
#define NS_B 2
#define NT_A 0
#define NT_B 1
#define OMAP 0
#define FTWAIT 0
#define QSCHED 1
#define PSLEEP 1
#define FSLEEP 4
#define NOBOX 0
#define PRECLR 0
#define OROT ((ob + ocb) & 7)
#define WPRE 0
#define WMID 0
#define WROT (ocb + (ob >> 1))
#define BOXPOLL 1
#define WL 0
#define WT0 8
#define EARLYPOLL 0
#define FS 32
#define OUTAUX 16
#define LATE23 0
#define SMPOS 2
#define KPOS 5
#define VPOS 7
#define TROT 0
#define PRETOUCH 0
#define QBUF 3
#define NSTAMP 18
#define NWAVE 4
#define STAMP_OFF (64u << 20)
#define TAKE(k) c.st_[k] = __builtin_amdgcn_s_memrealtime()

DI unsigned rfl(unsigned x) { return __builtin_amdgcn_readfirstlane(x); }
DI rsrc_t mk_rsrc(const void* p, unsigned bytes) { return __builtin_amdgcn_make_buffer_rsrc((void*)p, 0, bytes, 0x00020000); }
DI unsigned cvtpk(float lo, float hi) { f32x2 v = {lo, hi}; bf16x2 b = __builtin_convertvector(v, bf16x2); return __builtin_bit_cast(unsigned, b); }
DI void bar() { asm volatile("s_waitcnt lgkmcnt(0)\n\ts_barrier" ::: "memory"); }
template <int N> DI void vmwait() { asm volatile("s_waitcnt vmcnt(%0)" :: "n"(N) : "memory"); }
#define ESTAMP(k)
#define CONTIG 0
#define TOFF(t) (((((t) + c.tile0) & (NT - 1)) * 2 + c.kh) * 8192u)
#define VSHIFT 0
#define SBAR() __builtin_amdgcn_sched_barrier(0)
#define SEGT(i)

#define DMA_TILE(NTS) asm volatile( \
    "s_waitcnt lgkmcnt(0)\n\t" \
    "s_mov_b32 m0, %0\n\ts_nop 0\n\t" \
    "buffer_load_dwordx4 %5, %4, %2 offen" NTS " lds\n\t" \
    "buffer_load_dwordx4 %6, %4, %2 offen offset:1024" NTS " lds\n\t" \
    "buffer_load_dwordx4 %7, %4, %2 offen offset:2048" NTS " lds\n\t" \
    "buffer_load_dwordx4 %8, %4, %2 offen offset:3072" NTS " lds\n\t" \
    "s_mov_b32 m0, %1\n\ts_nop 0\n\t" \
    "buffer_load_dwordx4 %5, %4, %3 offen" NTS " lds\n\t" \
    "buffer_load_dwordx4 %6, %4, %3 offen offset:1024" NTS " lds\n\t" \
    "buffer_load_dwordx4 %7, %4, %3 offen offset:2048" NTS " lds\n\t" \
    "buffer_load_dwordx4 %8, %4, %3 offen offset:3072" NTS " lds" \
    :: "s"(lds), "s"(lds + 4096u), "s"(soff), "s"(soff + 4096u), "s"(srd), "v"(vo[0]), "v"(vo[1]), "v"(vo[2]), "v"(vo[3]) : "memory")
DI void dma_tile(bool nt, unsigned lds, rsrc_t srd, unsigned soff, const unsigned (&vo)[4]) {
  if (nt) DMA_TILE(" nt"); else DMA_TILE("");
}
DI u32x4 bload(rsrc_t srd, unsigned voff, unsigned soff) {
  u32x4 r; asm volatile("buffer_load_dwordx4 %0, %1, %2, %3 offen" : "=v"(r) : "v"(voff), "s"(srd), "s"(soff) : "memory"); return r;
}
DI u32x4 bload_sc1(rsrc_t srd, unsigned voff, unsigned soff) {
  u32x4 r; asm volatile("buffer_load_dwordx4 %0, %1, %2, %3 offen sc1" : "=v"(r) : "v"(voff), "s"(srd), "s"(soff) : "memory"); return r;
}

struct Ctx { LAS char* smem; unsigned lds0, ring; rsrc_t srdK, srdV, srdWo; unsigned vo[4], wvo, wso, tile0; int lane, kh, orot; bool nt;
  mutable unsigned long long st_[NSTAMP];
  mutable unsigned ts; mutable unsigned long long last;
};

template <int NS> DI void qproj(const Ctx& c, rsrc_t srdX, rsrc_t srdW, int head, int xrot, int wrot, f32x4 (&acc)[8][2]) {
  const int cc = c.lane & 15, g = c.lane >> 4;
  u32x4 wr[8][8];
  const unsigned wvo = g * 65536 + 16 * cc;
  const unsigned xvo = (c.lane >> 5) * 1024 + 16 * ((c.lane & 31) ^ (c.lane >> 5));
  const unsigned ximg = c.ring + 16384;
#define XISSUE(I0, I1) _Pragma("unroll") for (int i = (I0); i < (I1); ++i) { const unsigned p = (unsigned)(i + 4 * xrot) & 15u; \
    asm volatile("s_mov_b32 m0, %0\n\ts_nop 0\n\tbuffer_load_dwordx4 %1, %2, %3 offen lds" :: "s"(c.lds0 + ximg + 1024u * p), "v"(xvo ^ (32u * (p & 7u))), "s"(srdX), "s"(p * 2048u + c.kh * 512u) : "memory"); }
#define QISSUE(ks) do { const unsigned kk = (unsigned)(c.kh * 8 + (((ks) + wrot) & 7)); \
    _Pragma("unroll") for (int i = 0; i < 8; ++i) wr[ks][i] = bload(srdW, wvo, (kk * 32 + i) * 8192 + head * 256); } while (0)
  TAKE(8);
  static_assert((KPOS < 0 || KPOS >= 7 - QBUF) && (VPOS < 0 || VPOS >= 7 - QBUF) && KPOS <= VPOS, "positions");
  XISSUE(0, 4); QISSUE(0);
  if (KPOS < 0) dma_tile(c.nt, c.lds0 + c.ring, c.srdK, TOFF(0), c.vo);
  if (VPOS < 0) dma_tile(c.nt, c.lds0 + c.ring + 8192u, c.srdV, TOFF(0), c.vo);
  XISSUE(4, 16);
  TAKE(9);
#pragma unroll
  for (int ks = 1; ks < QBUF; ++ks) QISSUE(ks);
#pragma unroll
  for (int ks = 0; ks < 8; ++ks) {
    SBAR();
    const int nq = (ks + QBUF - 1 < 7 ? ks + QBUF - 1 : 7) - ks;
    const int n = nq * 8 + (KPOS >= 4 && ks > KPOS ? 8 : 0) + (VPOS >= 4 && ks > VPOS ? 8 : 0);
    asm volatile("s_waitcnt vmcnt(%8)" : "+v"(wr[ks][0]), "+v"(wr[ks][1]), "+v"(wr[ks][2]), "+v"(wr[ks][3]), "+v"(wr[ks][4]), "+v"(wr[ks][5]), "+v"(wr[ks][6]), "+v"(wr[ks][7]) : "n"(n) : "memory");
    TAKE(10 + ks);
    if (ks + QBUF < 8) QISSUE((ks + QBUF) & 7);
    SBAR();
    bf16x8 xf[2];
    const unsigned ch = 4u * ((ks + wrot) & 7) + g;
#pragma unroll
    for (int qb = 0; qb < 2; ++qb) xf[qb] = *(LAS bf16x8*)(c.smem + ximg + (cc + 16 * (qb ^ c.kh)) * 512 + 16 * (ch ^ cc));
    if (NS == 4 && ks == 7 && !LATE23) {
      if (VPOS == 7) dma_tile(c.nt, c.lds0 + c.ring + 8192u, c.srdV, TOFF(0), c.vo);
      dma_tile(c.nt, c.lds0 + c.ring + 16384u, c.srdK, TOFF(1), c.vo);
      dma_tile(c.nt, c.lds0 + c.ring + 24576u, c.srdV, TOFF(1), c.vo);
      SBAR();
    }
#pragma unroll
    for (int j = 0; j < 8; ++j) {
      u32x4 f;
#pragma unroll
      for (int m = 0; m < 4; ++m) f[m] = __builtin_amdgcn_perm(wr[ks][2 * m + 1][j >> 1], wr[ks][2 * m][j >> 1], (j & 1) ? 0x07060302u : 0x05040100u);
#pragma unroll
      for (int qb = 0; qb < 2; ++qb)
        acc[j][qb] = __builtin_amdgcn_mfma_f32_16x16x32_bf16(__builtin_bit_cast(bf16x8, f), xf[qb], ks ? acc[j][qb] : f32x4{0.f, 0.f, 0.f, 0.f}, 0, 0, 0);
    }
    __builtin_amdgcn_sched_group_barrier(0x100, 2, 0);
    __builtin_amdgcn_sched_group_barrier(0x2, 8, 0);
#pragma unroll
    for (int j = 0; j < 12; ++j) { __builtin_amdgcn_sched_group_barrier(0x8, 1, 0); __builtin_amdgcn_sched_group_barrier(0x2, 2, 0); }
    __builtin_amdgcn_sched_group_barrier(0x8, 4, 0);
    SBAR();
    if (ks == KPOS) dma_tile(c.nt, c.lds0 + c.ring, c.srdK, TOFF(0), c.vo);
    if (ks == VPOS && !(NS == 4 && ks == 7 && !LATE23)) dma_tile(c.nt, c.lds0 + c.ring + 8192u, c.srdV, TOFF(0), c.vo);
    if (NS == 4 && ks == 7 && LATE23) {
      dma_tile(c.nt, c.lds0 + c.ring + 16384u, c.srdK, TOFF(1), c.vo);
      if (LATE23 == 1) dma_tile(c.nt, c.lds0 + c.ring + 24576u, c.srdV, TOFF(1), c.vo);
    }
  }
#undef XISSUE
#undef QISSUE
}

#define DMA_W(I0, I1) _Pragma("unroll") for (int i = (I0); i < (I1); ++i) \
    asm volatile("s_mov_b32 m0, %0\n\ts_nop 0\n\tbuffer_load_dwordx4 %1, %2, %3 offen lds" :: "s"(c.lds0 + c.ring + 1024u * ((i + 16) & 31)), "v"(c.wvo), "s"(c.srdWo), \
                 "s"((unsigned)((((i >> 2) + c.orot) & 7) * 32 + (i & 3) * 8) * 1024u + c.wso) : "memory")

template <int NS> DI void attn_loop(const Ctx& c, const bf16x8 (&qf)[8], f32x16 (&o)[4], float& m, float& l) {
  const int lane = c.lane, r32 = lane & 31, hi = lane >> 5;
  const unsigned kaddr = c.ring + 256 * r32 + 16 * (hi ^ (((r32 & 3) << 2) | ((r32 >> 2) & 3)));
  const int blk = (lane >> 4) & 1, q4 = (lane & 15) >> 2, p4 = lane & 3;
  const unsigned vaddr = c.ring + 8192 + 256 * (4 * hi + q4) + 64 * q4 + 16 * ((2 * blk + (p4 & 1)) ^ hi) + 8 * (p4 >> 1);
  const unsigned vx = c.kh * 128u;
  static_assert(NS == 2 || NS == 4, "ring");
  constexpr int LA = NS / 2;
#pragma unroll 1
  for (int t0 = 0; t0 < NT; t0 += LA) {
#pragma unroll
    for (int u = 0; u < LA; ++u) {
      const int t = t0 + u;
      const unsigned so = u * 16384u;
      const bool more = t + LA < NT;
      const unsigned soffn = TOFF(t + LA);
      SEGT(0);
      if (more || u == 0) vmwait<(NS - 1) * 8>(); else vmwait<8>();
      SEGT(1);
      bf16x8 kf[8];
#pragma unroll
      for (int s = 0; s < 8; ++s) kf[s] = *(LAS bf16x8*)(c.smem + (kaddr ^ (32u * s)) + so);
      SEGT(2);
      if (more) dma_tile(c.nt, c.lds0 + c.ring + so, c.srdK, soffn, c.vo);
      SEGT(3);
      f32x16 sc = {};
#pragma unroll
      for (int s = 0; s < 8; ++s) sc = __builtin_amdgcn_mfma_f32_32x32x16_bf16(kf[s], qf[s], sc, 0, 0, 0);
      float mx = sc[0];
#pragma unroll
      for (int i = 1; i < 16; ++i) mx = fmaxf(mx, sc[i]);
      { auto rr = __builtin_amdgcn_permlane32_swap(__float_as_uint(mx), __float_as_uint(mx), false, false); mx = fmaxf(__uint_as_float(rr[0]), __uint_as_float(rr[1])); }
      const float mn = fmaxf(m, mx), alpha = __builtin_amdgcn_exp2f((m - mn) * SC2), nm = -mn * SC2;
      m = mn;
      float ps = 0.f;
#pragma unroll
      for (int i = 0; i < 16; ++i) { sc[i] = __builtin_amdgcn_exp2f(fmaf(sc[i], SC2, nm)); ps += sc[i]; }
      l = fmaf(l, alpha, ps);
      bf16x8 pf[2];
#pragma unroll
      for (int s = 0; s < 2; ++s) pf[s] = __builtin_bit_cast(bf16x8, u32x4{cvtpk(sc[8 * s], sc[8 * s + 1]), cvtpk(sc[8 * s + 2], sc[8 * s + 3]), cvtpk(sc[8 * s + 4], sc[8 * s + 5]), cvtpk(sc[8 * s + 6], sc[8 * s + 7])});
      asm volatile("" : "+v"(pf[0]), "+v"(pf[1]), "+v"(l));
#pragma unroll
      for (int cb = 0; cb < 4; ++cb) o[cb] *= alpha;
      asm volatile("" : "+v"(o[0]), "+v"(o[1]), "+v"(o[2]), "+v"(o[3]));
      SEGT(4);
      if (more) vmwait<(NS - 1) * 8>(); else if (u == 0) vmwait<(NS - 2) * 8>(); else vmwait<0>();
      SEGT(5);
      bf16x8 vf[4][2];
#pragma unroll
      for (int cb = 0; cb < 4; ++cb)
#pragma unroll
        for (int s = 0; s < 2; ++s) {
          const bf16x4 lo = __builtin_amdgcn_ds_read_tr16_b64_v4bf16((LAS bf16x4*)(c.smem + ((vaddr ^ vx ^ (64u * cb)) + 4096u * s + so)));
          const bf16x4 up = __builtin_amdgcn_ds_read_tr16_b64_v4bf16((LAS bf16x4*)(c.smem + ((vaddr ^ vx ^ (64u * cb) ^ 32u) + 4096u * s + 2048u + so)));
          vf[cb][s] = __builtin_shufflevector(lo, up, 0, 1, 2, 3, 4, 5, 6, 7);
        }
      SEGT(6);
      if (more) dma_tile(c.nt, c.lds0 + c.ring + 8192u + so, c.srdV, soffn, c.vo);
      SEGT(7);
#pragma unroll
      for (int s = 0; s < 2; ++s)
#pragma unroll
        for (int cb = 0; cb < 4; ++cb) o[cb] = __builtin_amdgcn_mfma_f32_32x32x16_bf16(vf[cb][s], pf[s], o[cb], 0, 0, 0);
    }
  }
}

__global__ __launch_bounds__(256, 2) void fused_kernel(const void* __restrict__ xA, const void* __restrict__ kA, const void* __restrict__ vA, const void* __restrict__ wqA, const void* __restrict__ woA,
                                                       const void* __restrict__ xB, const void* __restrict__ kB, const void* __restrict__ vB, const void* __restrict__ wqB, const void* __restrict__ woB,
                                                       char* ws, __hip_bfloat16* __restrict__ out) {
  extern __shared__ __attribute__((aligned(16))) char smem_[];
  Ctx c;
  c.smem = (LAS char*)smem_; c.lds0 = (unsigned)(uintptr_t)c.smem;
  LAS char* smem = c.smem;
  const int lane = threadIdx.x & 63;
  const int wave = rfl(threadIdx.x >> 6), br = (wave >> 1) ^ (A_OLD ^ 1);
  for (int k = 0; k < NSTAMP; ++k) c.st_[k] = 0;
  TAKE(0);
  c.lane = lane; c.kh = wave & 1; c.nt = br ? NT_B : NT_A; c.ring = wave * 32768u;
  const int g8 = blockIdx.x & 7, jj = blockIdx.x >> 3, b = jj >> 2, head = 4 * g8 + (jj & 3);
  const size_t kvoff = (size_t)(br ? b * 32 + head : b * 8 + g8) * (SK * DH * 2);
  c.srdK = mk_rsrc((const char*)(br ? kB : kA) + kvoff, SK * DH * 2);
  c.srdV = mk_rsrc((const char*)(br ? vB : vA) + kvoff, SK * DH * 2);
  const rsrc_t srdW = mk_rsrc(br ? wqB : wqA, DM * NH * DH * 2);
  const rsrc_t srdX = mk_rsrc((const char*)(br ? xB : xA) + (size_t)b * (SQ * DM * 2), SQ * DM * 2);
#pragma unroll
  for (int i = 0; i < 4; ++i) c.vo[i] = (lane >> 4) * 256 + 16 * ((lane & 15) ^ (((lane >> 4) << 2) | i));

  c.tile0 = TROT == 0 ? 0u : TROT == 1 ? (unsigned)((br ? blockIdx.x * 11 : (b * 8 + g8) * 5 + 2) & 31) : (unsigned)(((br ? blockIdx.x : b * 8 + g8 + 300) * 2654435761u) >> 27);
  const int obr = g8 >> 2, okq = g8 & 3, ob = 2 * (jj >> 3), ocb = jj & 7, h0 = okq * 8 + ((wave + WROT) & 3) * 2;
  c.orot = OROT;
  c.srdWo = mk_rsrc(obr ? woB : woA, NH * DH * DM * 2);
  c.wvo = (lane >> 3) * 1024 + 16 * ((lane & 7) ^ (((lane >> 4) & 1) << 2));
  c.wso = h0 * 128 * 1024 + ocb * 128;
  bf16x8 qf[8];
  {
    f32x4 acc[8][2];
    qproj<NS_A>(c, srdX, srdW, head, jj & 3, b, acc);
    TAKE(1);
    LAS f32x4* mine = (LAS f32x4*)(smem + SCR + wave * 8192) + lane;
#pragma unroll
    for (int j = 0; j < 8; ++j) mine[j * 64] = acc[j][1];
    if (LATE23 == 2 && NS_A == 4) dma_tile(c.nt, c.lds0 + c.ring + 24576u, c.srdV, TOFF(1), c.vo);
    bar();
    LAS f32x4* other = (LAS f32x4*)(smem + SCR + (wave ^ 1) * 8192) + lane;
#pragma unroll
    for (int j = 0; j < 8; ++j) acc[j][0] += other[j * 64];
    bar();
    LAS char* qimg = smem + SCR + (wave >> 1) * 8192;
    const int cc = lane & 15, g = lane >> 4, q = cc + 16 * c.kh;
#pragma unroll
    for (int e = 0; e < 4; ++e)
      *(LAS u32x4*)(qimg + q * 256 + 16 * ((4 * g + e) ^ cc)) = u32x4{cvtpk(acc[0][0][e], acc[1][0][e]), cvtpk(acc[2][0][e], acc[3][0][e]), cvtpk(acc[4][0][e], acc[5][0][e]), cvtpk(acc[6][0][e], acc[7][0][e])};
    bar();
#pragma unroll
    for (int s = 0; s < 8; ++s) qf[s] = *(LAS bf16x8*)(qimg + (lane & 31) * 256 + 16 * ((2 * s + (lane >> 5)) ^ (lane & 15)));
  }
  const unsigned long long tag64 = *(const __attribute__((address_space(4))) unsigned long long*)(ws + WS_GEN) + 1ull;
  const unsigned tag = (unsigned)tag64;
  TAKE(2);

  f32x16 o[4] = {};
  float m = -__builtin_inff(), l = 0.f;
  attn_loop<NS_A>(c, qf, o, m, l);
  TAKE(3);
  if (lane == 0) for (int k = 0; k < NSTAMP; ++k) __hip_atomic_store((GAS unsigned long long*)(ws + STAMP_OFF) + (blockIdx.x * 4 + wave) * NSTAMP + k, c.st_[k], __ATOMIC_RELAXED, __HIP_MEMORY_SCOPE_AGENT);

  GAS unsigned long long* gen = (GAS unsigned long long*)(ws + WS_GEN);
  GAS unsigned* flags = (GAS unsigned*)(ws + WS_FLAG);

  const int r32 = lane & 31, hi = lane >> 5;
  { auto rr = __builtin_amdgcn_permlane32_swap(__float_as_uint(l), __float_as_uint(l), false, false); l = __uint_as_float(rr[0]) + __uint_as_float(rr[1]); }
  {
    LAS f32x4* mine = (LAS f32x4*)(smem + SCR + wave * 8192) + lane;
    LAS f32x4* other = (LAS f32x4*)(smem + SCR + (wave ^ 1) * 8192) + lane;
    if (hi == 0) *(LAS f32x2*)(smem + c.ring + 15360 + r32 * 8) = f32x2{m, l};
#pragma unroll
    for (int u = 0; u < 8; ++u) mine[u * 64] = f32x4{o[2 + (u >> 2)][4 * (u & 3)], o[2 + (u >> 2)][4 * (u & 3) + 1], o[2 + (u >> 2)][4 * (u & 3) + 2], o[2 + (u >> 2)][4 * (u & 3) + 3]};
    ESTAMP(8);
    bar();
    ESTAMP(9);
    const f32x2 v = *(LAS f32x2*)(smem + (c.ring ^ 32768u) + 15360 + r32 * 8);
    const float ms = fmaxf(m, v[0]), e0 = __builtin_amdgcn_exp2f((m - ms) * SC2), e1 = __builtin_amdgcn_exp2f((v[0] - ms) * SC2);
    const float inv = 1.f / fmaf(e0, l, e1 * v[1]), f0 = e0 * inv, f1 = e1 * inv;
    const rsrc_t srdOw = mk_rsrc(ws + WS_O + (size_t)((br * NB + b) * NH + head) * 8192, 8192);
#pragma unroll
    for (int u = 0; u < 4; ++u) {
      const f32x4 a0 = other[(2 * u) * 64] * f1, a1 = other[(2 * u + 1) * 64] * f1;
      const int cb = u >> 1, r0 = 8 * (u & 1);
      const u32x4 w = {cvtpk(fmaf(o[cb][r0], f0, a0[0]), fmaf(o[cb][r0 + 1], f0, a0[1])), cvtpk(fmaf(o[cb][r0 + 2], f0, a0[2]), fmaf(o[cb][r0 + 3], f0, a0[3])),
                       cvtpk(fmaf(o[cb][r0 + 4], f0, a1[0]), fmaf(o[cb][r0 + 5], f0, a1[1])), cvtpk(fmaf(o[cb][r0 + 6], f0, a1[2]), fmaf(o[cb][r0 + 7], f0, a1[3]))};
      __builtin_amdgcn_raw_buffer_store_b128(w, srdOw, lane * 16, (c.kh * 4 + u) * 1024, 16);
    }
  }
  asm volatile("s_waitcnt lgkmcnt(0)" ::: "memory");
  ESTAMP(10);
  ESTAMP(11);
  vmwait<0>();
  ESTAMP(12);
  bar();
  if (threadIdx.x < 2) __hip_atomic_store(flags + ((threadIdx.x * NB + b) * NH + head) * FS, tag, __ATOMIC_RELAXED, __HIP_MEMORY_SCOPE_AGENT);
  const int blk = (lane >> 4) & 1, q4 = (lane & 15) >> 2, p4 = lane & 3;
  const unsigned waddr = c.ring + 128 * (8 * hi + q4) + 64 * (q4 >> 1) + 32 * blk + 8 * p4;
  const rsrc_t srdO = mk_rsrc(ws + WS_O + (size_t)((obr * NB + ob) * NH + h0) * 8192, NH * 8192 + 2 * 8192);
  GAS unsigned* fp = flags + ((obr * NB + ob + ((lane >> 1) & 1)) * NH + h0 + (lane & 1)) * FS;
  f32x16 acc[4] = {};
#define RD_W(S0, S1) _Pragma("unroll") for (int s = (S0); s < (S1); ++s) _Pragma("unroll") for (int nb = 0; nb < 2; ++nb) { \
    const bf16x4 lo = __builtin_amdgcn_ds_read_tr16_b64_v4bf16((LAS bf16x4*)(smem + (waddr ^ (64u * nb)) + 2048u * ((s + 8) & 15))); \
    const bf16x4 up = __builtin_amdgcn_ds_read_tr16_b64_v4bf16((LAS bf16x4*)(smem + (waddr ^ (64u * nb)) + 2048u * ((s + 8) & 15) + 512u)); \
    wf[s][nb] = __builtin_shufflevector(lo, up, 0, 1, 2, 3, 4, 5, 6, 7); }
#define O_ISSUE(S0, S1) _Pragma("unroll") for (int s = (S0); s < (S1); ++s) _Pragma("unroll") for (int bi = 0; bi < 2; ++bi) \
    of[s][bi] = bload_sc1(srdO, lane * 16, (unsigned)((s + 2 * c.orot) & 15) * 1024u + bi * (NH * 8192u))
#define O_WAIT(S, N) asm volatile("s_waitcnt vmcnt(%4)" : "+v"(of[S][0]), "+v"(of[S][1]), "+v"(of[(S) + 1][0]), "+v"(of[(S) + 1][1]) : "n"(N) : "memory")
#define O_MFMA(S0) _Pragma("unroll") for (int s = (S0); s < (S0) + 2; ++s) _Pragma("unroll") for (int j = 0; j < 4; ++j) \
    acc[j] = __builtin_amdgcn_mfma_f32_32x32x16_bf16(wf[s][j & 1], __builtin_bit_cast(bf16x8, of[s][j >> 1]), acc[j], 0, 0, 0)
  bool ready = false;
  DMA_W(0, 8);
  if (EARLYPOLL == 2 && ready) {
    TAKE(4); TAKE(5);
    u32x4 of[16][2]; bf16x8 wf[16][2];
    O_ISSUE(0, 4); DMA_W(8, 16);
#pragma unroll
    for (int s0 = 0; s0 < 16; s0 += 2) {
      SBAR();
      if (s0 == 0) O_WAIT(0, 12); else if (s0 == 2) O_WAIT(2, 16); else if (s0 == 4) O_WAIT(4, 12); else if (s0 == 6) O_WAIT(6, 12);
      else if (s0 == 8) O_WAIT(8, 12); else if (s0 == 10) O_WAIT(10, 8); else if (s0 == 12) O_WAIT(12, 4); else O_WAIT(14, 0);
      RD_W(s0, s0 + 2);
      if (s0 + 4 < 16) O_ISSUE(s0 + 4, s0 + 6);
      if (s0 < 8) DMA_W(2 * s0 + 16, 2 * s0 + 20);
      SBAR();
      O_MFMA(s0);
    }
  } else {
    DMA_W(8, 32);
    TAKE(4);
    if (!ready) {
      _Pragma("unroll 1") for (unsigned spins = 0; spins < SPIN_CAP; ++spins) {
        if (__all(__hip_atomic_load(fp, __ATOMIC_RELAXED, __HIP_MEMORY_SCOPE_AGENT) == tag)) break;
        __builtin_amdgcn_s_sleep(FSLEEP);
      }
      asm volatile("" ::: "memory");
    }
    TAKE(5);
    u32x4 of[16][2]; bf16x8 wf[16][2];
    O_ISSUE(0, 12);
    vmwait<24>();
    RD_W(0, 4);
#pragma unroll
    for (int s0 = 0; s0 < 16; s0 += 2) {
      SBAR();
      O_WAIT(s0, s0 < 4 ? 20 : 28 - 2 * s0);
      if (s0 < 4) O_ISSUE(12 + s0, 14 + s0);
      if (s0 + 4 < 16) RD_W(s0 + 4, s0 + 6);
      SBAR();
      O_MFMA(s0);
    }
  }
  TAKE(6);
#pragma unroll
  for (int u = 0; u < 16; ++u) *((LAS f32x4*)(smem + c.ring) + u * 64 + lane) = f32x4{acc[u >> 2][4 * (u & 3)], acc[u >> 2][4 * (u & 3) + 1], acc[u >> 2][4 * (u & 3) + 2], acc[u >> 2][4 * (u & 3) + 3]};
  bar();
  ESTAMP(13);
  {
    const unsigned thi = (unsigned)(tag64 >> 12), tlo = tag << 20;
    f32x4 a;
#pragma unroll
    for (int r = 3; r >= 0; --r) {
      const LAS f32x4* red = (const LAS f32x4*)smem + ((okq ^ r) * 4 + wave) * 64 + lane;
      a = (red[0] + red[2048]) + (red[4096] + red[6144]);
      if (r && !NOBOX) {
        const rsrc_t srdB = mk_rsrc(ws + WS_BOX + (size_t)(blockIdx.x ^ r) * 32768, 32768);
#pragma unroll
        for (int e = 0; e < 2; ++e) {
          const unsigned u0 = __float_as_uint(a[2 * e]), u1 = __float_as_uint(a[2 * e + 1]);
          const u32x4 w = {tlo | ((u0 + 0x7ffu + ((u0 >> 12) & 1u)) >> 12), thi, tlo | ((u1 + 0x7ffu + ((u1 >> 12) & 1u)) >> 12), thi};
          __builtin_amdgcn_raw_buffer_store_b128(w, srdB, threadIdx.x * 16, (r * 2 + e) * 4096, 16);
        }
      }
    }
    ESTAMP(14);
    const rsrc_t srdI = mk_rsrc(ws + WS_BOX + (size_t)blockIdx.x * 32768, 32768);
    u32x4 g[6] = {};
#define G_OK(i) ((g[i][1] == thi) & (g[i][3] == thi) & ((((g[i][0] ^ tlo) | (g[i][2] ^ tlo)) >> 20) == 0))
    _Pragma("unroll 1") for (unsigned spins = 0; spins < (NOBOX ? 0 : SPIN_CAP); ++spins) {
      bool ok = true;
#pragma unroll
      for (int i = 0; i < 6; ++i) g[i] = __builtin_amdgcn_raw_buffer_load_b128(srdI, threadIdx.x * 16, (2 + i) * 4096, 16);
#pragma unroll
      for (int i = 0; i < 6; ++i) ok &= G_OK(i);
      if (__all(ok)) break;
      asm volatile("s_sleep %0" :: "n"(PSLEEP) : "memory");
    }
    ESTAMP(16);
#pragma unroll
    for (int i = 0; i < 6; ++i) { a[2 * (i & 1)] += __uint_as_float(g[i][0] << 12); a[2 * (i & 1) + 1] += __uint_as_float(g[i][2] << 12); }
    __builtin_amdgcn_raw_buffer_store_b64(u32x2{cvtpk(a[0], a[1]), cvtpk(a[2], a[3])}, mk_rsrc(out, NB * SQ * 2048), r32 * 2048 + 8 * hi,
                                          (ob + (okq >> 1)) * SQ * 2048 + obr * 1024 + ocb * 128 + (okq & 1) * 64 + 16 * wave, OUTAUX);
  }
  if (blockIdx.x == 0 && wave == 0) {
    _Pragma("unroll 1") for (unsigned spins = 0; spins < SPIN_CAP; ++spins) {
      bool ok = true;
#pragma unroll
      for (int i = 0; i < 8; ++i) ok &= __hip_atomic_load(flags + (i * 64 + lane) * FS, __ATOMIC_RELAXED, __HIP_MEMORY_SCOPE_AGENT) == tag;
      if (__all(ok)) break;
      __builtin_amdgcn_s_sleep(1);
    }
    if (lane == 0) __hip_atomic_store(gen, tag64, __ATOMIC_RELAXED, __HIP_MEMORY_SCOPE_AGENT);
  }
  TAKE(7);
  if (blockIdx.x == 0 && wave == 0) {
    GAS unsigned long long* st = (GAS unsigned long long*)(ws + STAMP_OFF);
    unsigned long long mn = ~0ull, mk = 0;
    for (int w = lane; w < 1024; w += 64) { const unsigned long long a = __hip_atomic_load(st + w * NSTAMP, __ATOMIC_RELAXED, __HIP_MEMORY_SCOPE_AGENT), e = __hip_atomic_load(st + w * NSTAMP + DIAGK, __ATOMIC_RELAXED, __HIP_MEMORY_SCOPE_AGENT);
      mn = a < mn ? a : mn; mk = e > mk ? e : mk; }
    for (int d = 1; d < 64; d <<= 1) { unsigned long long a = __shfl_xor(mn, d), e = __shfl_xor(mk, d); mn = a < mn ? a : mn; mk = e > mk ? e : mk; }
    const unsigned long long X = DIAGK != 7 ? mk - mn : c.st_[7] - mn;
    const unsigned long long t1_ = __builtin_amdgcn_s_memrealtime(), until = t1_ + DIAGM * (X > 100000 ? 100000 : X); while (__builtin_amdgcn_s_memrealtime() < until) __builtin_amdgcn_s_sleep(2);
  }
}

extern "C" void kernel_launch(void* const* d_in, const int* in_sizes, int n_in, void* d_out, int out_size, void* d_ws, size_t ws_size, hipStream_t stream) {
  (void)in_sizes; (void)n_in; (void)out_size; (void)ws_size;
  (void)hipFuncSetAttribute((const void*)fused_kernel, hipFuncAttributeMaxDynamicSharedMemorySize, LDS1);
  hipLaunchKernelGGL(fused_kernel, dim3(256), dim3(256), LDS1, stream, d_in[0], d_in[1], d_in[2], d_in[3], d_in[4], d_in[5], d_in[6], d_in[7], d_in[8], d_in[9],
                     (char*)d_ws, (__hip_bfloat16*)d_out);
}
```

```cpp
#define DIAGA 0
#define DIAGB 16
#define DIAGM 0

#define STAMPS
#define ESTAMPS
#define DIAG
#include <hip/hip_runtime.h>
#include <hip/hip_bf16.h>
#include <stdint.h>

#define DI __device__ __forceinline__
#define LAS __attribute__((address_space(3)))
#define GAS __attribute__((address_space(1)))

typedef __attribute__((ext_vector_type(8))) __bf16 bf16x8;
typedef __attribute__((ext_vector_type(4))) __bf16 bf16x4;
typedef __attribute__((ext_vector_type(2))) __bf16 bf16x2;
typedef __attribute__((ext_vector_type(2))) float f32x2;
typedef __attribute__((ext_vector_type(4))) float f32x4;
typedef __attribute__((ext_vector_type(16))) float f32x16;
typedef __attribute__((ext_vector_type(4))) unsigned u32x4;
typedef __attribute__((ext_vector_type(2))) unsigned u32x2;
typedef __amdgpu_buffer_rsrc_t rsrc_t;

constexpr int NB = 8, SQ = 32, DM = 512, NH = 32, SK = 2048, DH = 128;
constexpr int NT = 32;
constexpr unsigned SCR = 131072;
constexpr unsigned LDS1 = 163840;
constexpr float SC2 = 0.08838834764831845f * 1.4426950408889634f;
constexpr unsigned WS_GEN = 0, WS_FLAG = 4096, WS_O = 262144, WS_BOX = WS_O + (4u << 20);
constexpr unsigned SPIN_CAP = 1u << 17;

#define A_OLD 1
#define NS_A 2
#define NS_B 2
#define NT_A 0
#define NT_B 1
#define OMAP 0
#define FTWAIT 0
#define QSCHED 1
#define PSLEEP 1
#define FSLEEP 4
#define NOBOX 0
#define PRECLR 0
#define OROT ((ob + ocb) & 7)
#define WPRE 0
#define WMID 0
#define WROT (ocb + (ob >> 1))
#define BOXPOLL 1
#define WL 0
#define WT0 8
#define EARLYPOLL 0
#define FS 32
#define OUTAUX 16
#define LATE23 0
#define SMPOS 2
#define KPOS 5
#define VPOS 7
#define TROT 0
#define PRETOUCH 0
#define QBUF 3
#define NSTAMP 18
#define NWAVE 4
#define STAMP_OFF (64u << 20)
#define TAKE(k) c.st_[k] = __builtin_amdgcn_s_memrealtime()

DI unsigned rfl(unsigned x) { return __builtin_amdgcn_readfirstlane(x); }
DI rsrc_t mk_rsrc(const void* p, unsigned bytes) { return __builtin_amdgcn_make_buffer_rsrc((void*)p, 0, bytes, 0x00020000); }
DI unsigned cvtpk(float lo, float hi) { f32x2 v = {lo, hi}; bf16x2 b = __builtin_convertvector(v, bf16x2); return __builtin_bit_cast(unsigned, b); }
DI void bar() { asm volatile("s_waitcnt lgkmcnt(0)\n\ts_barrier" ::: "memory"); }
template <int N> DI void vmwait() { asm volatile("s_waitcnt vmcnt(%0)" :: "n"(N) : "memory"); }
#define ESTAMP(k) TAKE(k)
#define CONTIG 0
#define TOFF(t) (((((t) + c.tile0) & (NT - 1)) * 2 + c.kh) * 8192u)
#define VSHIFT 0
#define SBAR() __builtin_amdgcn_sched_barrier(0)
#define SEGT(i)

#define DMA_TILE(NTS) asm volatile( \
    "s_waitcnt lgkmcnt(0)\n\t" \
    "s_mov_b32 m0, %0\n\ts_nop 0\n\t" \
    "buffer_load_dwordx4 %5, %4, %2 offen" NTS " lds\n\t" \
    "buffer_load_dwordx4 %6, %4, %2 offen offset:1024" NTS " lds\n\t" \
    "buffer_load_dwordx4 %7, %4, %2 offen offset:2048" NTS " lds\n\t" \
    "buffer_load_dwordx4 %8, %4, %2 offen offset:3072" NTS " lds\n\t" \
    "s_mov_b32 m0, %1\n\ts_nop 0\n\t" \
    "buffer_load_dwordx4 %5, %4, %3 offen" NTS " lds\n\t" \
    "buffer_load_dwordx4 %6, %4, %3 offen offset:1024" NTS " lds\n\t" \
    "buffer_load_dwordx4 %7, %4, %3 offen offset:2048" NTS " lds\n\t" \
    "buffer_load_dwordx4 %8, %4, %3 offen offset:3072" NTS " lds" \
    :: "s"(lds), "s"(lds + 4096u), "s"(soff), "s"(soff + 4096u), "s"(srd), "v"(vo[0]), "v"(vo[1]), "v"(vo[2]), "v"(vo[3]) : "memory")
DI void dma_tile(bool nt, unsigned lds, rsrc_t srd, unsigned soff, const unsigned (&vo)[4]) {
  if (nt) DMA_TILE(" nt"); else DMA_TILE("");
}
DI u32x4 bload(rsrc_t srd, unsigned voff, unsigned soff) {
  u32x4 r; asm volatile("buffer_load_dwordx4 %0, %1, %2, %3 offen" : "=v"(r) : "v"(voff), "s"(srd), "s"(soff) : "memory"); return r;
}
DI u32x4 bload_sc1(rsrc_t srd, unsigned voff, unsigned soff) {
  u32x4 r; asm volatile("buffer_load_dwordx4 %0, %1, %2, %3 offen sc1" : "=v"(r) : "v"(voff), "s"(srd), "s"(soff) : "memory"); return r;
}

struct Ctx { LAS char* smem; unsigned lds0, ring; rsrc_t srdK, srdV, srdWo; unsigned vo[4], wvo, wso, tile0; int lane, kh, orot; bool nt;
  mutable unsigned long long st_[NSTAMP];
  mutable unsigned ts; mutable unsigned long long last;
};

template <int NS> DI void qproj(const Ctx& c, rsrc_t srdX, rsrc_t srdW, int head, int xrot, int wrot, f32x4 (&acc)[8][2]) {
  const int cc = c.lane & 15, g = c.lane >> 4;
  u32x4 wr[8][8];
  const unsigned wvo = g * 65536 + 16 * cc;
  const unsigned xvo = (c.lane >> 5) * 1024 + 16 * ((c.lane & 31) ^ (c.lane >> 5));
  const unsigned ximg = c.ring + 16384;
#define XISSUE(I0, I1) _Pragma("unroll") for (int i = (I0); i < (I1); ++i) { const unsigned p = (unsigned)(i + 4 * xrot) & 15u; \
    asm volatile("s_mov_b32 m0, %0\n\ts_nop 0\n\tbuffer_load_dwordx4 %1, %2, %3 offen lds" :: "s"(c.lds0 + ximg + 1024u * p), "v"(xvo ^ (32u * (p & 7u))), "s"(srdX), "s"(p * 2048u + c.kh * 512u) : "memory"); }
#define QISSUE(ks) do { const unsigned kk = (unsigned)(c.kh * 8 + (((ks) + wrot) & 7)); \
    _Pragma("unroll") for (int i = 0; i < 8; ++i) wr[ks][i] = bload(srdW, wvo, (kk * 32 + i) * 8192 + head * 256); } while (0)
  static_assert((KPOS < 0 || KPOS >= 7 - QBUF) && (VPOS < 0 || VPOS >= 7 - QBUF) && KPOS <= VPOS, "positions");
  XISSUE(0, 4); QISSUE(0);
  if (KPOS < 0) dma_tile(c.nt, c.lds0 + c.ring, c.srdK, TOFF(0), c.vo);
  if (VPOS < 0) dma_tile(c.nt, c.lds0 + c.ring + 8192u, c.srdV, TOFF(0), c.vo);
  XISSUE(4, 16);
#pragma unroll
  for (int ks = 1; ks < QBUF; ++ks) QISSUE(ks);
#pragma unroll
  for (int ks = 0; ks < 8; ++ks) {
    SBAR();
    const int nq = (ks + QBUF - 1 < 7 ? ks + QBUF - 1 : 7) - ks;
    const int n = nq * 8 + (KPOS >= 4 && ks > KPOS ? 8 : 0) + (VPOS >= 4 && ks > VPOS ? 8 : 0);
    asm volatile("s_waitcnt vmcnt(%8)" : "+v"(wr[ks][0]), "+v"(wr[ks][1]), "+v"(wr[ks][2]), "+v"(wr[ks][3]), "+v"(wr[ks][4]), "+v"(wr[ks][5]), "+v"(wr[ks][6]), "+v"(wr[ks][7]) : "n"(n) : "memory");
    if (ks + QBUF < 8) QISSUE((ks + QBUF) & 7);
    SBAR();
    bf16x8 xf[2];
    const unsigned ch = 4u * ((ks + wrot) & 7) + g;
#pragma unroll
    for (int qb = 0; qb < 2; ++qb) xf[qb] = *(LAS bf16x8*)(c.smem + ximg + (cc + 16 * (qb ^ c.kh)) * 512 + 16 * (ch ^ cc));
    if (NS == 4 && ks == 7 && !LATE23) {
      if (VPOS == 7) dma_tile(c.nt, c.lds0 + c.ring + 8192u, c.srdV, TOFF(0), c.vo);
      dma_tile(c.nt, c.lds0 + c.ring + 16384u, c.srdK, TOFF(1), c.vo);
      dma_tile(c.nt, c.lds0 + c.ring + 24576u, c.srdV, TOFF(1), c.vo);
      SBAR();
    }
#pragma unroll
    for (int j = 0; j < 8; ++j) {
      u32x4 f;
#pragma unroll
      for (int m = 0; m < 4; ++m) f[m] = __builtin_amdgcn_perm(wr[ks][2 * m + 1][j >> 1], wr[ks][2 * m][j >> 1], (j & 1) ? 0x07060302u : 0x05040100u);
#pragma unroll
      for (int qb = 0; qb < 2; ++qb)
        acc[j][qb] = __builtin_amdgcn_mfma_f32_16x16x32_bf16(__builtin_bit_cast(bf16x8, f), xf[qb], ks ? acc[j][qb] : f32x4{0.f, 0.f, 0.f, 0.f}, 0, 0, 0);
    }
    __builtin_amdgcn_sched_group_barrier(0x100, 2, 0);
    __builtin_amdgcn_sched_group_barrier(0x2, 8, 0);
#pragma unroll
    for (int j = 0; j < 12; ++j) { __builtin_amdgcn_sched_group_barrier(0x8, 1, 0); __builtin_amdgcn_sched_group_barrier(0x2, 2, 0); }
    __builtin_amdgcn_sched_group_barrier(0x8, 4, 0);
    SBAR();
    if (ks == KPOS) dma_tile(c.nt, c.lds0 + c.ring, c.srdK, TOFF(0), c.vo);
    if (ks == VPOS && !(NS == 4 && ks == 7 && !LATE23)) dma_tile(c.nt, c.lds0 + c.ring + 8192u, c.srdV, TOFF(0), c.vo);
    if (NS == 4 && ks == 7 && LATE23) {
      dma_tile(c.nt, c.lds0 + c.ring + 16384u, c.srdK, TOFF(1), c.vo);
      if (LATE23 == 1) dma_tile(c.nt, c.lds0 + c.ring + 24576u, c.srdV, TOFF(1), c.vo);
    }
  }
#undef XISSUE
#undef QISSUE
}

#define DMA_W(I0, I1) _Pragma("unroll") for (int i = (I0); i < (I1); ++i) \
    asm volatile("s_mov_b32 m0, %0\n\ts_nop 0\n\tbuffer_load_dwordx4 %1, %2, %3 offen lds" :: "s"(c.lds0 + c.ring + 1024u * ((i + 16) & 31)), "v"(c.wvo), "s"(c.srdWo), \
                 "s"((unsigned)((((i >> 2) + c.orot) & 7) * 32 + (i & 3) * 8) * 1024u + c.wso) : "memory")

template <int NS> DI void attn_loop(const Ctx& c, const bf16x8 (&qf)[8], f32x16 (&o)[4], float& m, float& l) {
  const int lane = c.lane, r32 = lane & 31, hi = lane >> 5;
  const unsigned kaddr = c.ring + 256 * r32 + 16 * (hi ^ (((r32 & 3) << 2) | ((r32 >> 2) & 3)));
  const int blk = (lane >> 4) & 1, q4 = (lane & 15) >> 2, p4 = lane & 3;
  const unsigned vaddr = c.ring + 8192 + 256 * (4 * hi + q4) + 64 * q4 + 16 * ((2 * blk + (p4 & 1)) ^ hi) + 8 * (p4 >> 1);
  const unsigned vx = c.kh * 128u;
  static_assert(NS == 2 || NS == 4, "ring");
  constexpr int LA = NS / 2;
#pragma unroll 1
  for (int t0 = 0; t0 < NT; t0 += LA) {
#pragma unroll
    for (int u = 0; u < LA; ++u) {
      const int t = t0 + u;
      const unsigned so = u * 16384u;
      const bool more = t + LA < NT;
      const unsigned soffn = TOFF(t + LA);
      SEGT(0);
      if (more || u == 0) vmwait<(NS - 1) * 8>(); else vmwait<8>();
      SEGT(1);
      bf16x8 kf[8];
#pragma unroll
      for (int s = 0; s < 8; ++s) kf[s] = *(LAS bf16x8*)(c.smem + (kaddr ^ (32u * s)) + so);
      SEGT(2);
      if (more) dma_tile(c.nt, c.lds0 + c.ring + so, c.srdK, soffn, c.vo);
      SEGT(3);
      f32x16 sc = {};
#pragma unroll
      for (int s = 0; s < 8; ++s) sc = __builtin_amdgcn_mfma_f32_32x32x16_bf16(kf[s], qf[s], sc, 0, 0, 0);
      float mx = sc[0];
#pragma unroll
      for (int i = 1; i < 16; ++i) mx = fmaxf(mx, sc[i]);
      { auto rr = __builtin_amdgcn_permlane32_swap(__float_as_uint(mx), __float_as_uint(mx), false, false); mx = fmaxf(__uint_as_float(rr[0]), __uint_as_float(rr[1])); }
      const float mn = fmaxf(m, mx), alpha = __builtin_amdgcn_exp2f((m - mn) * SC2), nm = -mn * SC2;
      m = mn;
      float ps = 0.f;
#pragma unroll
      for (int i = 0; i < 16; ++i) { sc[i] = __builtin_amdgcn_exp2f(fmaf(sc[i], SC2, nm)); ps += sc[i]; }
      l = fmaf(l, alpha, ps);
      bf16x8 pf[2];
#pragma unroll
      for (int s = 0; s < 2; ++s) pf[s] = __builtin_bit_cast(bf16x8, u32x4{cvtpk(sc[8 * s], sc[8 * s + 1]), cvtpk(sc[8 * s + 2], sc[8 * s + 3]), cvtpk(sc[8 * s + 4], sc[8 * s + 5]), cvtpk(sc[8 * s + 6], sc[8 * s + 7])});
      asm volatile("" : "+v"(pf[0]), "+v"(pf[1]), "+v"(l));
#pragma unroll
      for (int cb = 0; cb < 4; ++cb) o[cb] *= alpha;
      asm volatile("" : "+v"(o[0]), "+v"(o[1]), "+v"(o[2]), "+v"(o[3]));
      SEGT(4);
      if (more) vmwait<(NS - 1) * 8>(); else if (u == 0) vmwait<(NS - 2) * 8>(); else vmwait<0>();
      SEGT(5);
      bf16x8 vf[4][2];
#pragma unroll
      for (int cb = 0; cb < 4; ++cb)
#pragma unroll
        for (int s = 0; s < 2; ++s) {
          const bf16x4 lo = __builtin_amdgcn_ds_read_tr16_b64_v4bf16((LAS bf16x4*)(c.smem + ((vaddr ^ vx ^ (64u * cb)) + 4096u * s + so)));
          const bf16x4 up = __builtin_amdgcn_ds_read_tr16_b64_v4bf16((LAS bf16x4*)(c.smem + ((vaddr ^ vx ^ (64u * cb) ^ 32u) + 4096u * s + 2048u + so)));
          vf[cb][s] = __builtin_shufflevector(lo, up, 0, 1, 2, 3, 4, 5, 6, 7);
        }
      SEGT(6);
      if (more) dma_tile(c.nt, c.lds0 + c.ring + 8192u + so, c.srdV, soffn, c.vo);
      SEGT(7);
#pragma unroll
      for (int s = 0; s < 2; ++s)
#pragma unroll
        for (int cb = 0; cb < 4; ++cb) o[cb] = __builtin_amdgcn_mfma_f32_32x32x16_bf16(vf[cb][s], pf[s], o[cb], 0, 0, 0);
    }
  }
}

__global__ __launch_bounds__(256, 2) void fused_kernel(const void* __restrict__ xA, const void* __restrict__ kA, const void* __restrict__ vA, const void* __restrict__ wqA, const void* __restrict__ woA,
                                                       const void* __restrict__ xB, const void* __restrict__ kB, const void* __restrict__ vB, const void* __restrict__ wqB, const void* __restrict__ woB,
                                                       char* ws, __hip_bfloat16* __restrict__ out) {
  extern __shared__ __attribute__((aligned(16))) char smem_[];
  Ctx c;
  c.smem = (LAS char*)smem_; c.lds0 = (unsigned)(uintptr_t)c.smem;
  LAS char* smem = c.smem;
  const int lane = threadIdx.x & 63;
  const int wave = rfl(threadIdx.x >> 6), br = (wave >> 1) ^ (A_OLD ^ 1);
  for (int k = 0; k < NSTAMP; ++k) c.st_[k] = 0;
  TAKE(0);
  c.lane = lane; c.kh = wave & 1; c.nt = br ? NT_B : NT_A; c.ring = wave * 32768u;
  const int g8 = blockIdx.x & 7, jj = blockIdx.x >> 3, b = jj >> 2, head = 4 * g8 + (jj & 3);
  const size_t kvoff = (size_t)(br ? b * 32 + head : b * 8 + g8) * (SK * DH * 2);
  c.srdK = mk_rsrc((const char*)(br ? kB : kA) + kvoff, SK * DH * 2);
  c.srdV = mk_rsrc((const char*)(br ? vB : vA) + kvoff, SK * DH * 2);
  const rsrc_t srdW = mk_rsrc(br ? wqB : wqA, DM * NH * DH * 2);
  const rsrc_t srdX = mk_rsrc((const char*)(br ? xB : xA) + (size_t)b * (SQ * DM * 2), SQ * DM * 2);
#pragma unroll
  for (int i = 0; i < 4; ++i) c.vo[i] = (lane >> 4) * 256 + 16 * ((lane & 15) ^ (((lane >> 4) << 2) | i));

  c.tile0 = TROT == 0 ? 0u : TROT == 1 ? (unsigned)((br ? blockIdx.x * 11 : (b * 8 + g8) * 5 + 2) & 31) : (unsigned)(((br ? blockIdx.x : b * 8 + g8 + 300) * 2654435761u) >> 27);
  const int obr = g8 >> 2, okq = g8 & 3, ob = 2 * (jj >> 3), ocb = jj & 7, h0 = okq * 8 + ((wave + WROT) & 3) * 2;
  c.orot = OROT;
  c.srdWo = mk_rsrc(obr ? woB : woA, NH * DH * DM * 2);
  c.wvo = (lane >> 3) * 1024 + 16 * ((lane & 7) ^ (((lane >> 4) & 1) << 2));
  c.wso = h0 * 128 * 1024 + ocb * 128;
  bf16x8 qf[8];
  {
    f32x4 acc[8][2];
    qproj<NS_A>(c, srdX, srdW, head, jj & 3, b, acc);
    TAKE(1);
    LAS f32x4* mine = (LAS f32x4*)(smem + SCR + wave * 8192) + lane;
#pragma unroll
    for (int j = 0; j < 8; ++j) mine[j * 64] = acc[j][1];
    if (LATE23 == 2 && NS_A == 4) dma_tile(c.nt, c.lds0 + c.ring + 24576u, c.srdV, TOFF(1), c.vo);
    bar();
    LAS f32x4* other = (LAS f32x4*)(smem + SCR + (wave ^ 1) * 8192) + lane;
#pragma unroll
    for (int j = 0; j < 8; ++j) acc[j][0] += other[j * 64];
    bar();
    LAS char* qimg = smem + SCR + (wave >> 1) * 8192;
    const int cc = lane & 15, g = lane >> 4, q = cc + 16 * c.kh;
#pragma unroll
    for (int e = 0; e < 4; ++e)
      *(LAS u32x4*)(qimg + q * 256 + 16 * ((4 * g + e) ^ cc)) = u32x4{cvtpk(acc[0][0][e], acc[1][0][e]), cvtpk(acc[2][0][e], acc[3][0][e]), cvtpk(acc[4][0][e], acc[5][0][e]), cvtpk(acc[6][0][e], acc[7][0][e])};
    bar();
#pragma unroll
    for (int s = 0; s < 8; ++s) qf[s] = *(LAS bf16x8*)(qimg + (lane & 31) * 256 + 16 * ((2 * s + (lane >> 5)) ^ (lane & 15)));
  }
  const unsigned long long tag64 = *(const __attribute__((address_space(4))) unsigned long long*)(ws + WS_GEN) + 1ull;
  const unsigned tag = (unsigned)tag64;
  TAKE(2);

  f32x16 o[4] = {};
  float m = -__builtin_inff(), l = 0.f;
  attn_loop<NS_A>(c, qf, o, m, l);
  TAKE(3);

  GAS unsigned long long* gen = (GAS unsigned long long*)(ws + WS_GEN);
  GAS unsigned* flags = (GAS unsigned*)(ws + WS_FLAG);

  const int r32 = lane & 31, hi = lane >> 5;
  { auto rr = __builtin_amdgcn_permlane32_swap(__float_as_uint(l), __float_as_uint(l), false, false); l = __uint_as_float(rr[0]) + __uint_as_float(rr[1]); }
  {
    LAS f32x4* mine = (LAS f32x4*)(smem + SCR + wave * 8192) + lane;
    LAS f32x4* other = (LAS f32x4*)(smem + SCR + (wave ^ 1) * 8192) + lane;
    if (hi == 0) *(LAS f32x2*)(smem + c.ring + 15360 + r32 * 8) = f32x2{m, l};
#pragma unroll
    for (int u = 0; u < 8; ++u) mine[u * 64] = f32x4{o[2 + (u >> 2)][4 * (u & 3)], o[2 + (u >> 2)][4 * (u & 3) + 1], o[2 + (u >> 2)][4 * (u & 3) + 2], o[2 + (u >> 2)][4 * (u & 3) + 3]};
    ESTAMP(8);
    bar();
    ESTAMP(9);
    const f32x2 v = *(LAS f32x2*)(smem + (c.ring ^ 32768u) + 15360 + r32 * 8);
    const float ms = fmaxf(m, v[0]), e0 = __builtin_amdgcn_exp2f((m - ms) * SC2), e1 = __builtin_amdgcn_exp2f((v[0] - ms) * SC2);
    const float inv = 1.f / fmaf(e0, l, e1 * v[1]), f0 = e0 * inv, f1 = e1 * inv;
    const rsrc_t srdOw = mk_rsrc(ws + WS_O + (size_t)((br * NB + b) * NH + head) * 8192, 8192);
#pragma unroll
    for (int u = 0; u < 4; ++u) {
      const f32x4 a0 = other[(2 * u) * 64] * f1, a1 = other[(2 * u + 1) * 64] * f1;
      const int cb = u >> 1, r0 = 8 * (u & 1);
      const u32x4 w = {cvtpk(fmaf(o[cb][r0], f0, a0[0]), fmaf(o[cb][r0 + 1], f0, a0[1])), cvtpk(fmaf(o[cb][r0 + 2], f0, a0[2]), fmaf(o[cb][r0 + 3], f0, a0[3])),
                       cvtpk(fmaf(o[cb][r0 + 4], f0, a1[0]), fmaf(o[cb][r0 + 5], f0, a1[1])), cvtpk(fmaf(o[cb][r0 + 6], f0, a1[2]), fmaf(o[cb][r0 + 7], f0, a1[3]))};
      __builtin_amdgcn_raw_buffer_store_b128(w, srdOw, lane * 16, (c.kh * 4 + u) * 1024, 16);
    }
  }
  asm volatile("s_waitcnt lgkmcnt(0)" ::: "memory");
  ESTAMP(10);
  ESTAMP(11);
  vmwait<0>();
  ESTAMP(12);
  bar();
  if (threadIdx.x < 2) __hip_atomic_store(flags + ((threadIdx.x * NB + b) * NH + head) * FS, tag, __ATOMIC_RELAXED, __HIP_MEMORY_SCOPE_AGENT);
  const int blk = (lane >> 4) & 1, q4 = (lane & 15) >> 2, p4 = lane & 3;
  const unsigned waddr = c.ring + 128 * (8 * hi + q4) + 64 * (q4 >> 1) + 32 * blk + 8 * p4;
  const rsrc_t srdO = mk_rsrc(ws + WS_O + (size_t)((obr * NB + ob) * NH + h0) * 8192, NH * 8192 + 2 * 8192);
  GAS unsigned* fp = flags + ((obr * NB + ob + ((lane >> 1) & 1)) * NH + h0 + (lane & 1)) * FS;
  f32x16 acc[4] = {};
#define RD_W(S0, S1) _Pragma("unroll") for (int s = (S0); s < (S1); ++s) _Pragma("unroll") for (int nb = 0; nb < 2; ++nb) { \
    const bf16x4 lo = __builtin_amdgcn_ds_read_tr16_b64_v4bf16((LAS bf16x4*)(smem + (waddr ^ (64u * nb)) + 2048u * ((s + 8) & 15))); \
    const bf16x4 up = __builtin_amdgcn_ds_read_tr16_b64_v4bf16((LAS bf16x4*)(smem + (waddr ^ (64u * nb)) + 2048u * ((s + 8) & 15) + 512u)); \
    wf[s][nb] = __builtin_shufflevector(lo, up, 0, 1, 2, 3, 4, 5, 6, 7); }
#define O_ISSUE(S0, S1) _Pragma("unroll") for (int s = (S0); s < (S1); ++s) _Pragma("unroll") for (int bi = 0; bi < 2; ++bi) \
    of[s][bi] = bload_sc1(srdO, lane * 16, (unsigned)((s + 2 * c.orot) & 15) * 1024u + bi * (NH * 8192u))
#define O_WAIT(S, N) asm volatile("s_waitcnt vmcnt(%4)" : "+v"(of[S][0]), "+v"(of[S][1]), "+v"(of[(S) + 1][0]), "+v"(of[(S) + 1][1]) : "n"(N) : "memory")
#define O_MFMA(S0) _Pragma("unroll") for (int s = (S0); s < (S0) + 2; ++s) _Pragma("unroll") for (int j = 0; j < 4; ++j) \
    acc[j] = __builtin_amdgcn_mfma_f32_32x32x16_bf16(wf[s][j & 1], __builtin_bit_cast(bf16x8, of[s][j >> 1]), acc[j], 0, 0, 0)
  bool ready = false;
  DMA_W(0, 8);
  if (EARLYPOLL == 2 && ready) {
    TAKE(4); TAKE(5);
    u32x4 of[16][2]; bf16x8 wf[16][2];
    O_ISSUE(0, 4); DMA_W(8, 16);
#pragma unroll
    for (int s0 = 0; s0 < 16; s0 += 2) {
      SBAR();
      if (s0 == 0) O_WAIT(0, 12); else if (s0 == 2) O_WAIT(2, 16); else if (s0 == 4) O_WAIT(4, 12); else if (s0 == 6) O_WAIT(6, 12);
      else if (s0 == 8) O_WAIT(8, 12); else if (s0 == 10) O_WAIT(10, 8); else if (s0 == 12) O_WAIT(12, 4); else O_WAIT(14, 0);
      RD_W(s0, s0 + 2);
      if (s0 + 4 < 16) O_ISSUE(s0 + 4, s0 + 6);
      if (s0 < 8) DMA_W(2 * s0 + 16, 2 * s0 + 20);
      SBAR();
      O_MFMA(s0);
    }
  } else {
    DMA_W(8, 32);
    TAKE(4);
    if (!ready) {
      _Pragma("unroll 1") for (unsigned spins = 0; spins < SPIN_CAP; ++spins) {
        if (__all(__hip_atomic_load(fp, __ATOMIC_RELAXED, __HIP_MEMORY_SCOPE_AGENT) == tag)) break;
        __builtin_amdgcn_s_sleep(FSLEEP);
      }
      asm volatile("" ::: "memory");
    }
    TAKE(5);
    u32x4 of[16][2]; bf16x8 wf[16][2];
    O_ISSUE(0, 12);
    vmwait<24>();
    RD_W(0, 4);
#pragma unroll
    for (int s0 = 0; s0 < 16; s0 += 2) {
      SBAR();
      O_WAIT(s0, s0 < 4 ? 20 : 28 - 2 * s0);
      if (s0 < 4) O_ISSUE(12 + s0, 14 + s0);
      if (s0 + 4 < 16) RD_W(s0 + 4, s0 + 6);
      SBAR();
      O_MFMA(s0);
    }
  }
  TAKE(6);
#pragma unroll
  for (int u = 0; u < 16; ++u) *((LAS f32x4*)(smem + c.ring) + u * 64 + lane) = f32x4{acc[u >> 2][4 * (u & 3)], acc[u >> 2][4 * (u & 3) + 1], acc[u >> 2][4 * (u & 3) + 2], acc[u >> 2][4 * (u & 3) + 3]};
  bar();
  ESTAMP(13);
  {
    const unsigned thi = (unsigned)(tag64 >> 12), tlo = tag << 20;
    f32x4 a;
#pragma unroll
    for (int r = 3; r >= 0; --r) {
      const LAS f32x4* red = (const LAS f32x4*)smem + ((okq ^ r) * 4 + wave) * 64 + lane;
      a = (red[0] + red[2048]) + (red[4096] + red[6144]);
      if (r && !NOBOX) {
        const rsrc_t srdB = mk_rsrc(ws + WS_BOX + (size_t)(blockIdx.x ^ r) * 32768, 32768);
#pragma unroll
        for (int e = 0; e < 2; ++e) {
          const unsigned u0 = __float_as_uint(a[2 * e]), u1 = __float_as_uint(a[2 * e + 1]);
          const u32x4 w = {tlo | ((u0 + 0x7ffu + ((u0 >> 12) & 1u)) >> 12), thi, tlo | ((u1 + 0x7ffu + ((u1 >> 12) & 1u)) >> 12), thi};
          __builtin_amdgcn_raw_buffer_store_b128(w, srdB, threadIdx.x * 16, (r * 2 + e) * 4096, 16);
        }
      }
    }
    ESTAMP(14);
    const rsrc_t srdI = mk_rsrc(ws + WS_BOX + (size_t)blockIdx.x * 32768, 32768);
    u32x4 g[6] = {};
#define G_OK(i) ((g[i][1] == thi) & (g[i][3] == thi) & ((((g[i][0] ^ tlo) | (g[i][2] ^ tlo)) >> 20) == 0))
    _Pragma("unroll 1") for (unsigned spins = 0; spins < (NOBOX ? 0 : SPIN_CAP); ++spins) {
      bool ok = true;
#pragma unroll
      for (int i = 0; i < 6; ++i) g[i] = __builtin_amdgcn_raw_buffer_load_b128(srdI, threadIdx.x * 16, (2 + i) * 4096, 16);
#pragma unroll
      for (int i = 0; i < 6; ++i) ok &= G_OK(i);
      if (spins == 0) ESTAMP(15);
      if (__all(ok)) break;
      asm volatile("s_sleep %0" :: "n"(PSLEEP) : "memory");
    }
    ESTAMP(16);
#pragma unroll
    for (int i = 0; i < 6; ++i) { a[2 * (i & 1)] += __uint_as_float(g[i][0] << 12); a[2 * (i & 1) + 1] += __uint_as_float(g[i][2] << 12); }
    __builtin_amdgcn_raw_buffer_store_b64(u32x2{cvtpk(a[0], a[1]), cvtpk(a[2], a[3])}, mk_rsrc(out, NB * SQ * 2048), r32 * 2048 + 8 * hi,
                                          (ob + (okq >> 1)) * SQ * 2048 + obr * 1024 + ocb * 128 + (okq & 1) * 64 + 16 * wave, OUTAUX);
  }
  if (blockIdx.x == 0 && wave == 0) {
    _Pragma("unroll 1") for (unsigned spins = 0; spins < SPIN_CAP; ++spins) {
      bool ok = true;
#pragma unroll
      for (int i = 0; i < 8; ++i) ok &= __hip_atomic_load(flags + (i * 64 + lane) * FS, __ATOMIC_RELAXED, __HIP_MEMORY_SCOPE_AGENT) == tag;
      if (__all(ok)) break;
      __builtin_amdgcn_s_sleep(1);
    }
    if (lane == 0) __hip_atomic_store(gen, tag64, __ATOMIC_RELAXED, __HIP_MEMORY_SCOPE_AGENT);
  }
  TAKE(7);
  {
    const unsigned long long X = c.st_[DIAGB] - c.st_[DIAGA];
    const unsigned long long until = c.st_[16] + DIAGM * (X > 100000 ? 100000 : X); while (__builtin_amdgcn_s_memrealtime() < until) __builtin_amdgcn_s_sleep(2);
  }
}

extern "C" void kernel_launch(void* const* d_in, const int* in_sizes, int n_in, void* d_out, int out_size, void* d_ws, size_t ws_size, hipStream_t stream) {
  (void)in_sizes; (void)n_in; (void)out_size; (void)ws_size;
  (void)hipFuncSetAttribute((const void*)fused_kernel, hipFuncAttributeMaxDynamicSharedMemorySize, LDS1);
  hipLaunchKernelGGL(fused_kernel, dim3(256), dim3(256), LDS1, stream, d_in[0], d_in[1], d_in[2], d_in[3], d_in[4], d_in[5], d_in[6], d_in[7], d_in[8], d_in[9],
                     (char*)d_ws, (__hip_bfloat16*)d_out);
}
```

```cpp
#include <hip/hip_runtime.h>
#include <hip/hip_bf16.h>
#include <stdint.h>

#define DI __device__ __forceinline__
#define LAS __attribute__((address_space(3)))
#define GAS __attribute__((address_space(1)))
#define RLX_AGENT __ATOMIC_RELAXED, __HIP_MEMORY_SCOPE_AGENT

typedef __attribute__((ext_vector_type(8))) __bf16 bf16x8;
typedef __attribute__((ext_vector_type(4))) __bf16 bf16x4;
typedef __attribute__((ext_vector_type(2))) __bf16 bf16x2;
typedef __attribute__((ext_vector_type(2))) float f32x2;
typedef __attribute__((ext_vector_type(4))) float f32x4;
typedef __attribute__((ext_vector_type(16))) float f32x16;
typedef __attribute__((ext_vector_type(4))) unsigned u32x4;
typedef __attribute__((ext_vector_type(2))) unsigned u32x2;
typedef __amdgpu_buffer_rsrc_t rsrc_t;

constexpr int NB = 8, SQ = 32, DM = 512, NH = 32, HKV = 8, SK = 2048, DH = 128;
constexpr int NT = 32;
constexpr unsigned SCR = 131072;
constexpr unsigned LDS_BYTES = 163840;
constexpr float SC2 = 0.08838834764831845f * 1.4426950408889634f;
constexpr unsigned WS_GEN = 0, WS_SEEN = 1024, WS_FLAG = 4096, FS = 32, WS_O = 262144, WS_BOX = WS_O + (4u << 20);
constexpr unsigned SPIN_CAP = 1u << 17;
constexpr int QBUF = 3;
#define EARLY 2
#define EPOS 0
#define PATHI 1
#define PSLEEP "s_sleep 1"
#define FSLEEP 4
#define POLL2 24
#define WPRE 0
#define LAZY 6

#define TAKE(k)

DI unsigned rfl(unsigned x) { return __builtin_amdgcn_readfirstlane(x); }
DI rsrc_t mk_rsrc(const void* p, unsigned bytes) { return __builtin_amdgcn_make_buffer_rsrc((void*)p, 0, bytes, 0x00020000); }
DI unsigned cvtpk(float lo, float hi) { f32x2 v = {lo, hi}; bf16x2 b = __builtin_convertvector(v, bf16x2); return __builtin_bit_cast(unsigned, b); }
DI void bar() { asm volatile("s_waitcnt lgkmcnt(0)\n\ts_barrier" ::: "memory"); }
template <int N> DI void vmwait() { asm volatile("s_waitcnt vmcnt(%0)" :: "n"(N) : "memory"); }
#define SBAR() __builtin_amdgcn_sched_barrier(0)

#define TILE8(NTS) \
    "buffer_load_dwordx4 %6, %5, %3 offen" NTS " lds\n\t" \
    "buffer_load_dwordx4 %7, %5, %3 offen offset:1024" NTS " lds\n\t" \
    "buffer_load_dwordx4 %8, %5, %3 offen offset:2048" NTS " lds\n\t" \
    "buffer_load_dwordx4 %9, %5, %3 offen offset:3072" NTS " lds\n\t" \
    "s_mov_b32 m0, %2\n\ts_nop 0\n\t" \
    "buffer_load_dwordx4 %6, %5, %4 offen" NTS " lds\n\t" \
    "buffer_load_dwordx4 %7, %5, %4 offen offset:1024" NTS " lds\n\t" \
    "buffer_load_dwordx4 %8, %5, %4 offen offset:2048" NTS " lds\n\t" \
    "buffer_load_dwordx4 %9, %5, %4 offen offset:3072" NTS " lds\n\t"
DI void dma_tile(unsigned nt, unsigned lds, rsrc_t srd, unsigned soff, const unsigned (&vo)[4]) {
  asm volatile("s_mov_b32 m0, %1\n\ts_cmp_eq_u32 %0, 0\n\ts_waitcnt lgkmcnt(0)\n\ts_cbranch_scc1 .Ldef%=\n\t" TILE8(" nt") "s_branch .Lend%=\n.Ldef%=:\n\t" TILE8("") ".Lend%=:"
               :: "s"(nt), "s"(lds), "s"(lds + 4096u), "s"(soff), "s"(soff + 4096u), "s"(srd), "v"(vo[0]), "v"(vo[1]), "v"(vo[2]), "v"(vo[3]) : "memory", "scc");
}
DI void dma_piece(unsigned lds, rsrc_t srd, unsigned voff, unsigned soff) {
  asm volatile("s_mov_b32 m0, %0\n\ts_nop 0\n\tbuffer_load_dwordx4 %1, %2, %3 offen lds" :: "s"(lds), "v"(voff), "s"(srd), "s"(soff) : "memory");
}
DI u32x4 bload(rsrc_t srd, unsigned voff, unsigned soff) {
  u32x4 r; asm volatile("buffer_load_dwordx4 %0, %1, %2, %3 offen" : "=v"(r) : "v"(voff), "s"(srd), "s"(soff) : "memory"); return r;
}
DI u32x4 bload_sc1(rsrc_t srd, unsigned voff, unsigned soff) {
  u32x4 r; asm volatile("buffer_load_dwordx4 %0, %1, %2, %3 offen sc1" : "=v"(r) : "v"(voff), "s"(srd), "s"(soff) : "memory"); return r;
}

struct Att {
  unsigned ldsK, nt, kh; rsrc_t srdK, srdV; unsigned vo[4], ka[8], va[8];
  bf16x8 qf[8]; f32x16 o[4]; float m, l;
};

template <bool MORE> DI void take_k(Att& a, int t, bf16x8 (&kf)[8]) {
  vmwait<8>();
#pragma unroll
  for (int s = 0; s < 8; ++s) kf[s] = *(LAS bf16x8*)(uintptr_t)a.ka[s];
  SBAR();
  if (MORE) dma_tile(a.nt, a.ldsK, a.srdK, (unsigned)((t + 1) * 2 + a.kh) * 8192u, a.vo);
  SBAR();
}
template <bool MORE> DI void take_v(Att& a, int t, bf16x8 (&vf)[4][2]) {
  if (MORE) vmwait<8>(); else vmwait<0>();
#pragma unroll
  for (int cb = 0; cb < 4; ++cb)
#pragma unroll
    for (int s = 0; s < 2; ++s) {
      const bf16x4 lo = __builtin_amdgcn_ds_read_tr16_b64_v4bf16((LAS bf16x4*)(uintptr_t)a.va[2 * cb] + 512 * s);
      const bf16x4 up = __builtin_amdgcn_ds_read_tr16_b64_v4bf16((LAS bf16x4*)(uintptr_t)a.va[2 * cb + 1] + 512 * s + 256);
      vf[cb][s] = __builtin_shufflevector(lo, up, 0, 1, 2, 3, 4, 5, 6, 7);
    }
  SBAR();
  if (MORE) dma_tile(a.nt, a.ldsK + 8192u, a.srdV, (unsigned)((t + 1) * 2 + a.kh) * 8192u, a.vo);
  SBAR();
}
template <bool FIRST, bool MORE> DI void attn_tile(Att& a, int t, bf16x8 (&kf)[8], bf16x8 (&vf)[4][2]) {
  if (!FIRST || EARLY < 1) take_k<MORE>(a, t, kf);
  f32x16 sc = {};
#pragma unroll
  for (int s = 0; s < 8; ++s) sc = __builtin_amdgcn_mfma_f32_32x32x16_bf16(kf[s], a.qf[s], sc, 0, 0, 0);
  float mx = sc[0];
#pragma unroll
  for (int i = 1; i < 16; ++i) mx = fmaxf(mx, sc[i]);
  { auto rr = __builtin_amdgcn_permlane32_swap(__float_as_uint(mx), __float_as_uint(mx), false, false); mx = fmaxf(__uint_as_float(rr[0]), __uint_as_float(rr[1])); }
  if (FIRST) a.m = mx;
  else if (LAZY == 0 || __builtin_expect(__any((mx - a.m) * SC2 > (float)LAZY), 0)) {
    const float mn = fmaxf(a.m, mx), alpha = __builtin_amdgcn_exp2f((a.m - mn) * SC2);
    a.m = mn; a.l *= alpha;
#pragma unroll
    for (int cb = 0; cb < 4; ++cb) a.o[cb] *= alpha;
  }
  const float nm = -a.m * SC2;
  float ps = 0.f;
#pragma unroll
  for (int i = 0; i < 16; ++i) { sc[i] = __builtin_amdgcn_exp2f(fmaf(sc[i], SC2, nm)); ps += sc[i]; }
  a.l = FIRST ? ps : a.l + ps;
  bf16x8 pf[2];
#pragma unroll
  for (int s = 0; s < 2; ++s)
    pf[s] = __builtin_bit_cast(bf16x8, u32x4{cvtpk(sc[8 * s], sc[8 * s + 1]), cvtpk(sc[8 * s + 2], sc[8 * s + 3]), cvtpk(sc[8 * s + 4], sc[8 * s + 5]), cvtpk(sc[8 * s + 6], sc[8 * s + 7])});
  asm volatile("" : "+v"(pf[0]), "+v"(pf[1]), "+v"(a.l));
  if (!FIRST || EARLY < 2) take_v<MORE>(a, t, vf);
#pragma unroll
  for (int s = 0; s < 2; ++s)
#pragma unroll
    for (int cb = 0; cb < 4; ++cb) a.o[cb] = __builtin_amdgcn_mfma_f32_32x32x16_bf16(vf[cb][s], pf[s], FIRST && s == 0 ? f32x16{} : a.o[cb], 0, 0, 0);
}

__global__ __launch_bounds__(256, 2) void fused_kernel(const void* __restrict__ xA, const void* __restrict__ kA, const void* __restrict__ vA, const void* __restrict__ wqA, const void* __restrict__ woA,
                                                       const void* __restrict__ xB, const void* __restrict__ kB, const void* __restrict__ vB, const void* __restrict__ wqB, const void* __restrict__ woB,
                                                       char* ws, __hip_bfloat16* __restrict__ out) {
  extern __shared__ __attribute__((aligned(16))) char smem_[];
  LAS char* smem = (LAS char*)smem_;
  const unsigned lds0 = (unsigned)(uintptr_t)smem;
  const int lane = threadIdx.x & 63, r32 = lane & 31, hi = lane >> 5;
  const int wave = rfl(threadIdx.x >> 6), br = wave >> 1, kh = wave & 1;
  const unsigned ring = wave * 32768u;
  const int g8 = blockIdx.x & 7, jj = blockIdx.x >> 3, b = jj >> 2, head = 4 * g8 + (jj & 3);
  Att a;
  a.ldsK = lds0 + ring; a.nt = br; a.kh = kh;
  const size_t kvoff = (size_t)(br ? b * NH + head : b * HKV + g8) * (SK * DH * 2);
  a.srdK = mk_rsrc((const char*)(br ? kB : kA) + kvoff, SK * DH * 2);
  a.srdV = mk_rsrc((const char*)(br ? vB : vA) + kvoff, SK * DH * 2);
#pragma unroll
  for (int i = 0; i < 4; ++i) a.vo[i] = (lane >> 4) * 256 + 16 * ((lane & 15) ^ (((lane >> 4) << 2) | i));

  bf16x8 kf0[8], vf0[4][2];
  {
    const rsrc_t srdW = mk_rsrc(br ? wqB : wqA, DM * NH * DH * 2);
    const rsrc_t srdX = mk_rsrc((const char*)(br ? xB : xA) + (size_t)b * (SQ * DM * 2), SQ * DM * 2);
    const int cc = lane & 15, g = lane >> 4;
    const unsigned wvo = g * 65536 + 16 * cc, xvo = hi * 1024 + 16 * (r32 ^ hi), ximg = ring + 16384;
    u32x4 wr[8][8];
    f32x4 acc[8][2];
#define XISSUE(I0, I1) _Pragma("unroll") for (int i = (I0); i < (I1); ++i) { const unsigned p = (unsigned)(i + 4 * jj) & 15u; \
      dma_piece(lds0 + ximg + 1024u * p, srdX, xvo ^ (32u * (p & 7u)), p * 2048u + kh * 512u); }
#define QISSUE(ks) do { const unsigned kk = (unsigned)(kh * 8 + (((ks) + b) & 7)); \
      _Pragma("unroll") for (int i = 0; i < 8; ++i) wr[ks][i] = bload(srdW, wvo, (kk * 32 + i) * 8192 + head * 256); } while (0)
    XISSUE(0, 4); QISSUE(0); XISSUE(4, 16);
#pragma unroll
    for (int ks = 1; ks < QBUF; ++ks) QISSUE(ks);
    {
      const unsigned kaddr = ring + 256 * r32 + 16 * (hi ^ (((r32 & 3) << 2) | ((r32 >> 2) & 3)));
      const int blk = (lane >> 4) & 1, q4 = (lane & 15) >> 2, p4 = lane & 3;
      const unsigned vaddr = (ring + 8192 + 256 * (4 * hi + q4) + 64 * q4 + 16 * ((2 * blk + (p4 & 1)) ^ hi) + 8 * (p4 >> 1)) ^ (kh * 128u);
#pragma unroll
      for (int s = 0; s < 8; ++s) { a.ka[s] = lds0 + (kaddr ^ (32u * s)); a.va[s] = lds0 + (vaddr ^ (32u * s)); }
      asm volatile("" : "+v"(a.ka[0]), "+v"(a.ka[1]), "+v"(a.ka[2]), "+v"(a.ka[3]), "+v"(a.ka[4]), "+v"(a.ka[5]), "+v"(a.ka[6]), "+v"(a.ka[7]),
                        "+v"(a.va[0]), "+v"(a.va[1]), "+v"(a.va[2]), "+v"(a.va[3]), "+v"(a.va[4]), "+v"(a.va[5]), "+v"(a.va[6]), "+v"(a.va[7]));
    }
#pragma unroll
    for (int ks = 0; ks < 8; ++ks) {
      SBAR();
      const int n = ((ks + QBUF - 1 < 7 ? ks + QBUF - 1 : 7) - ks) * 8 + (ks > 5 ? 8 : 0);
      asm volatile("s_waitcnt vmcnt(%8)" : "+v"(wr[ks][0]), "+v"(wr[ks][1]), "+v"(wr[ks][2]), "+v"(wr[ks][3]), "+v"(wr[ks][4]), "+v"(wr[ks][5]), "+v"(wr[ks][6]), "+v"(wr[ks][7]) : "n"(n) : "memory");
      if (ks + QBUF < 8) QISSUE((ks + QBUF) & 7);
      SBAR();
      bf16x8 xf[2];
      const unsigned ch = 4u * ((ks + b) & 7) + g;
#pragma unroll
      for (int qb = 0; qb < 2; ++qb) xf[qb] = *(LAS bf16x8*)(smem + ximg + (cc + 16 * (qb ^ kh)) * 512 + 16 * (ch ^ cc));
#pragma unroll
      for (int j = 0; j < 8; ++j) {
        u32x4 f;
#pragma unroll
        for (int m = 0; m < 4; ++m) f[m] = __builtin_amdgcn_perm(wr[ks][2 * m + 1][j >> 1], wr[ks][2 * m][j >> 1], (j & 1) ? 0x07060302u : 0x05040100u);
#pragma unroll
        for (int qb = 0; qb < 2; ++qb)
          acc[j][qb] = __builtin_amdgcn_mfma_f32_16x16x32_bf16(__builtin_bit_cast(bf16x8, f), xf[qb], ks ? acc[j][qb] : f32x4{0.f, 0.f, 0.f, 0.f}, 0, 0, 0);
      }
      __builtin_amdgcn_sched_group_barrier(0x100, 2, 0);
      __builtin_amdgcn_sched_group_barrier(0x2, 8, 0);
#pragma unroll
      for (int j = 0; j < 12; ++j) { __builtin_amdgcn_sched_group_barrier(0x8, 1, 0); __builtin_amdgcn_sched_group_barrier(0x2, 2, 0); }
      __builtin_amdgcn_sched_group_barrier(0x8, 4, 0);
      SBAR();
      if (ks == 5) dma_tile(a.nt, a.ldsK, a.srdK, kh * 8192u, a.vo);
      if (ks == 7) dma_tile(a.nt, a.ldsK + 8192u, a.srdV, kh * 8192u, a.vo);
    }
#undef XISSUE
#undef QISSUE
    TAKE(1);
    LAS f32x4* mine = (LAS f32x4*)(smem + SCR + wave * 8192) + lane;
#pragma unroll
    for (int j = 0; j < 8; ++j) mine[j * 64] = acc[j][1];
    if (EARLY >= 1 && EPOS == 0) take_k<true>(a, 0, kf0);
    bar();
    LAS f32x4* other = (LAS f32x4*)(smem + SCR + (wave ^ 1) * 8192) + lane;
#pragma unroll
    for (int j = 0; j < 8; ++j) acc[j][0] += other[j * 64];
    LAS char* qimg = smem + (wave & 2) * 32768 + 16384;
#pragma unroll
    for (int e = 0; e < 4; ++e)
      *(LAS u32x4*)(qimg + (cc + 16 * kh) * 256 + 16 * ((4 * g + e) ^ cc)) =
          u32x4{cvtpk(acc[0][0][e], acc[1][0][e]), cvtpk(acc[2][0][e], acc[3][0][e]), cvtpk(acc[4][0][e], acc[5][0][e]), cvtpk(acc[6][0][e], acc[7][0][e])};
    if (EARLY >= 1 && EPOS == 1) take_k<true>(a, 0, kf0);
    if (EARLY >= 2 && EPOS == 0) take_v<true>(a, 0, vf0);
    bar();
#pragma unroll
    for (int s = 0; s < 8; ++s) a.qf[s] = *(LAS bf16x8*)(qimg + r32 * 256 + 16 * ((2 * s + hi) ^ (lane & 15)));
    if (EARLY >= 2 && EPOS == 1) take_v<true>(a, 0, vf0);
  }
  TAKE(2);

  attn_tile<true, true>(a, 0, kf0, vf0);
#pragma unroll 1
  for (int t = 1; t < NT - 1; ++t) { bf16x8 kf[8], vf[4][2]; attn_tile<false, true>(a, t, kf, vf); }
  { bf16x8 kf[8], vf[4][2]; attn_tile<false, false>(a, NT - 1, kf, vf); }
  TAKE(3);

  unsigned long long gen = __hip_atomic_load((GAS unsigned long long*)(ws + WS_GEN), RLX_AGENT);
  GAS unsigned* flags = (GAS unsigned*)(ws + WS_FLAG);

  {
    { auto rr = __builtin_amdgcn_permlane32_swap(__float_as_uint(a.l), __float_as_uint(a.l), false, false); a.l = __uint_as_float(rr[0]) + __uint_as_float(rr[1]); }
    LAS f32x4* mine = (LAS f32x4*)(smem + SCR + wave * 8192) + lane;
    LAS f32x4* other = (LAS f32x4*)(smem + SCR + (wave ^ 1) * 8192) + lane;
    *(LAS f32x2*)(smem + ring + lane * 8) = f32x2{a.m, a.l};
#pragma unroll
    for (int u = 0; u < 8; ++u) mine[u * 64] = f32x4{a.o[2 + (u >> 2)][4 * (u & 3)], a.o[2 + (u >> 2)][4 * (u & 3) + 1], a.o[2 + (u >> 2)][4 * (u & 3) + 2], a.o[2 + (u >> 2)][4 * (u & 3) + 3]};
    bar();
    const f32x2 v = *(LAS f32x2*)(smem + (ring ^ 32768u) + lane * 8);
    const float ms = fmaxf(a.m, v[0]), e0 = __builtin_amdgcn_exp2f((a.m - ms) * SC2), e1 = __builtin_amdgcn_exp2f((v[0] - ms) * SC2);
    const float inv = __builtin_amdgcn_rcpf(fmaf(e0, a.l, e1 * v[1])), f0 = e0 * inv, f1 = e1 * inv;
    const rsrc_t srdOw = mk_rsrc(ws + WS_O + (size_t)((br * NB + b) * NH + head) * 8192, 8192);
#pragma unroll
    for (int u = 0; u < 4; ++u) {
      const f32x4 a0 = other[(2 * u) * 64] * f1, a1 = other[(2 * u + 1) * 64] * f1;
      const int cb = u >> 1, r0 = 8 * (u & 1);
      const u32x4 w = {cvtpk(fmaf(a.o[cb][r0], f0, a0[0]), fmaf(a.o[cb][r0 + 1], f0, a0[1])), cvtpk(fmaf(a.o[cb][r0 + 2], f0, a0[2]), fmaf(a.o[cb][r0 + 3], f0, a0[3])),
                       cvtpk(fmaf(a.o[cb][r0 + 4], f0, a1[0]), fmaf(a.o[cb][r0 + 5], f0, a1[1])), cvtpk(fmaf(a.o[cb][r0 + 6], f0, a1[2]), fmaf(a.o[cb][r0 + 7], f0, a1[3]))};
      __builtin_amdgcn_raw_buffer_store_b128(w, srdOw, lane * 16, (kh * 4 + u) * 1024, 16);
    }
  }
  vmwait<0>();
  asm volatile("" : "+v"(gen));
  const unsigned long long tag64 = (((unsigned long long)rfl((unsigned)(gen >> 32)) << 32) | rfl((unsigned)gen)) + 1ull;
  const unsigned tag = (unsigned)tag64;
  TAKE(12);
  bar();
  if (threadIdx.x < 3)
    __hip_atomic_store(threadIdx.x < 2 ? flags + ((threadIdx.x * NB + b) * NH + head) * FS : (GAS unsigned*)(ws + WS_SEEN) + blockIdx.x, tag, RLX_AGENT);

  const int obr = g8 >> 2, okq = g8 & 3, ob = 2 * (jj >> 3), ocb = jj & 7, h0 = okq * 8 + ((wave + ocb + (ob >> 1)) & 3) * 2, orot = (ob + ocb) & 7;
  f32x16 acc[4] = {};
  {
    const rsrc_t srdWo = mk_rsrc(obr ? woB : woA, NH * DH * DM * 2);
    const unsigned wvo = (lane >> 3) * 1024 + 16 * ((lane & 7) ^ (((lane >> 4) & 1) << 2));
    GAS unsigned* fp = flags + ((obr * NB + ob + ((lane >> 1) & 1)) * NH + h0 + (lane & 1)) * FS;
    const rsrc_t srdO = mk_rsrc(ws + WS_O + (size_t)((obr * NB + ob) * NH + h0) * 8192, NH * 8192 + 2 * 8192);
    const int blk = (lane >> 4) & 1, q4 = (lane & 15) >> 2, p4 = lane & 3;
    const unsigned waddr = (ring + 128 * (8 * hi + q4) + 64 * (q4 >> 1) + 32 * blk + 8 * p4) ^ (16u * wave) ^ (64u * (okq & 1));
#define DMA_W(I0, I1) _Pragma("unroll") for (int i = (I0); i < (I1); ++i)        \
      dma_piece(lds0 + ring + 1024u * i, srdWo, wvo, (unsigned)(h0 * 128 + (((i >> 2) + orot) & 7) * 32 + (i & 3) * 8) * 1024u + ocb * 128u)
#define RD_W(S0, S1) _Pragma("unroll") for (int s = (S0); s < (S1); ++s) _Pragma("unroll") for (int nb = 0; nb < 2; ++nb) { \
      const bf16x4 lo = __builtin_amdgcn_ds_read_tr16_b64_v4bf16((LAS bf16x4*)(smem + (waddr ^ (64u * nb)) + 2048u * s)); \
      const bf16x4 up = __builtin_amdgcn_ds_read_tr16_b64_v4bf16((LAS bf16x4*)(smem + (waddr ^ (64u * nb)) + 2048u * s + 512u)); \
      wf[s][nb] = __builtin_shufflevector(lo, up, 0, 1, 2, 3, 4, 5, 6, 7); }
#define O_ISSUE(S0, S1) _Pragma("unroll") for (int s = (S0); s < (S1); ++s) _Pragma("unroll") for (int bi = 0; bi < 2; ++bi) \
      of[s][bi] = bload_sc1(srdO, lane * 16, (unsigned)((s + 2 * orot) & 15) * 1024u + (bi ^ (okq >> 1)) * (NH * 8192u))
#define O_WAIT(S, N) asm volatile("s_waitcnt vmcnt(%4)" : "+v"(of[S][0]), "+v"(of[S][1]), "+v"(of[(S) + 1][0]), "+v"(of[(S) + 1][1]) : "n"(N) : "memory")
#define O_MFMA(S0) _Pragma("unroll") for (int s = (S0); s < (S0) + 2; ++s) _Pragma("unroll") for (int j = 0; j < 4; ++j) \
      acc[j] = __builtin_amdgcn_mfma_f32_32x32x16_bf16(wf[s][j & 1], __builtin_bit_cast(bf16x8, of[s][j >> 1]), acc[j], 0, 0, 0)
    bool ready = false;
    if (PATHI) {
      unsigned f0;
      asm volatile("global_load_dword %0, %1, off sc1" : "=v"(f0) : "v"(fp) : "memory");
      DMA_W(0, 8);
      asm volatile("s_waitcnt vmcnt(8)" : "+v"(f0) :: "memory");
      ready = __all(f0 == tag);
    } else { DMA_W(0, 8); }
    if (PATHI && ready) {
      TAKE(4); TAKE(5);
      u32x4 of[16][2]; bf16x8 wf[16][2];
      O_ISSUE(0, 4); DMA_W(8, 16);
#pragma unroll
      for (int s0 = 0; s0 < 16; s0 += 2) {
        SBAR();
        O_WAIT(s0, s0 == 2 ? 16 : s0 <= 8 ? 12 : 28 - 2 * s0);
        RD_W(s0, s0 + 2);
        if (s0 + 4 < 16) O_ISSUE(s0 + 4, s0 + 6);
        if (s0 < 8) DMA_W(2 * s0 + 16, 2 * s0 + 20);
        SBAR();
        O_MFMA(s0);
      }
    } else {
      unsigned f1;
      DMA_W(8, POLL2);
      asm volatile("global_load_dword %0, %1, off sc1" : "=v"(f1) : "v"(fp) : "memory");
      DMA_W(POLL2, 32);
      TAKE(4);
      asm volatile("s_waitcnt vmcnt(%1)" : "+v"(f1) : "n"(32 - POLL2) : "memory");
      if (!__all(f1 == tag)) {
#pragma unroll 1
        for (unsigned spins = 0; spins < SPIN_CAP; ++spins) {
          if (__all(__hip_atomic_load(fp, RLX_AGENT) == tag)) break;
          __builtin_amdgcn_s_sleep(FSLEEP);
        }
      }
      asm volatile("" ::: "memory");
      TAKE(5);
      {
        u32x4 of[16][2]; bf16x8 wf[16][2];
        O_ISSUE(0, 12);
        vmwait<24>();
        RD_W(0, 4);
#pragma unroll
        for (int s0 = 0; s0 < 16; s0 += 2) {
          SBAR();
          O_WAIT(s0, s0 < 4 ? 20 : 28 - 2 * s0);
          if (s0 < 4) O_ISSUE(12 + s0, 14 + s0);
          if (s0 + 4 < 16) RD_W(s0 + 4, s0 + 6);
          SBAR();
          O_MFMA(s0);
        }
      }
    }
#undef DMA_W
#undef RD_W
#undef O_ISSUE
#undef O_WAIT
#undef O_MFMA
  }
  TAKE(6);
#pragma unroll
  for (int u = 0; u < 16; ++u)
    if (u & 3) *((LAS f32x4*)(smem + ring) + u * 64 + lane) = f32x4{acc[u >> 2][4 * (u & 3)], acc[u >> 2][4 * (u & 3) + 1], acc[u >> 2][4 * (u & 3) + 2], acc[u >> 2][4 * (u & 3) + 3]};
  bar();
  TAKE(13);
  {
    const unsigned t0 = tag & 0x3fffffu, t1 = ((unsigned)(tag64 >> 22) ^ tag) & 0x3fffffu;
    f32x4 s;
#pragma unroll
    for (int r = 3; r >= 0; --r) {
      s = f32x4{acc[r][0], acc[r][1], acc[r][2], acc[r][3]};
#pragma unroll
      for (int x = 1; x < 4; ++x) s += *((const LAS f32x4*)(smem + (ring ^ (x * 32768u))) + (r * 4 + x) * 64 + lane);
      if (r) {
        const u32x4 u = __builtin_bit_cast(u32x4, s) + 0x400u;
        const u32x4 w = {__builtin_amdgcn_alignbit(u[1] >> 11, u[0], 11), __builtin_amdgcn_alignbit(t0, u[1], 22), __builtin_amdgcn_alignbit(u[3] >> 11, u[2], 11), __builtin_amdgcn_alignbit(t1, u[3], 22)};
        __builtin_amdgcn_raw_buffer_store_b128(w, mk_rsrc(ws + WS_BOX + (size_t)(blockIdx.x ^ r) * 16384, 16384), threadIdx.x * 16, r * 4096, 16);
      }
    }
    TAKE(14);
    const rsrc_t srdI = mk_rsrc(ws + WS_BOX + (size_t)blockIdx.x * 16384, 16384);
    u32x4 g[3];
#pragma unroll 1
    for (unsigned spins = 0; spins < SPIN_CAP; ++spins) {
      unsigned bad = 0;
#pragma unroll
      for (int i = 0; i < 3; ++i) g[i] = __builtin_amdgcn_raw_buffer_load_b128(srdI, threadIdx.x * 16, (1 + i) * 4096, 16);
#pragma unroll
      for (int i = 0; i < 3; ++i) bad |= (g[i][1] ^ (t0 << 10)) | (g[i][3] ^ (t1 << 10));
      if (__all(bad < 1024u)) break;
      asm volatile(PSLEEP ::: "memory");
    }
    TAKE(16);
#pragma unroll
    for (int i = 0; i < 3; ++i)
      s += __builtin_bit_cast(f32x4, u32x4{g[i][0], __builtin_amdgcn_alignbit(g[i][1], g[i][0], 21), g[i][2], __builtin_amdgcn_alignbit(g[i][3], g[i][2], 21)} << 11);
    __builtin_amdgcn_raw_buffer_store_b64(u32x2{cvtpk(s[0], s[1]), cvtpk(s[2], s[3])}, mk_rsrc(out, NB * SQ * 2048), r32 * 2048 + 8 * hi,
                                          (ob + (okq >> 1)) * SQ * 2048 + obr * 1024 + ocb * 128 + (okq & 1) * 64 + 16 * wave, 16);
  }
  if (blockIdx.x == 0 && wave == 0) {
    const rsrc_t srdS = mk_rsrc(ws + WS_SEEN, 1024);
#pragma unroll 1
    for (unsigned spins = 0; spins < SPIN_CAP; ++spins) {
      const u32x4 v = __builtin_amdgcn_raw_buffer_load_b128(srdS, lane * 16, 0, 16);
      if (__all(((v[0] ^ tag) | (v[1] ^ tag) | (v[2] ^ tag) | (v[3] ^ tag)) == 0)) break;
      asm volatile("s_sleep 1" ::: "memory");
    }
    if (lane == 0) __hip_atomic_store((GAS unsigned long long*)(ws + WS_GEN), tag64, RLX_AGENT);
  }
  TAKE(7);
}

extern "C" void kernel_launch(void* const* d_in, const int* in_sizes, int n_in, void* d_out, int out_size, void* d_ws, size_t ws_size, hipStream_t stream) {
  (void)in_sizes; (void)n_in; (void)out_size; (void)ws_size;
  (void)hipFuncSetAttribute((const void*)fused_kernel, hipFuncAttributeMaxDynamicSharedMemorySize, LDS_BYTES);
  hipLaunchKernelGGL(fused_kernel, dim3(256), dim3(256), LDS_BYTES, stream, d_in[0], d_in[1], d_in[2], d_in[3], d_in[4], d_in[5], d_in[6], d_in[7], d_in[8], d_in[9],
                     (char*)d_ws, (__hip_bfloat16*)d_out);
}
```

```cpp
#include <hip/hip_runtime.h>
#include <hip/hip_bf16.h>
#include <stdint.h>

#define DI __device__ __forceinline__
#define LAS __attribute__((address_space(3)))
#define GAS __attribute__((address_space(1)))
#define RLX_AGENT __ATOMIC_RELAXED, __HIP_MEMORY_SCOPE_AGENT

typedef __attribute__((ext_vector_type(8))) __bf16 bf16x8;
typedef __attribute__((ext_vector_type(4))) __bf16 bf16x4;
typedef __attribute__((ext_vector_type(2))) __bf16 bf16x2;
typedef __attribute__((ext_vector_type(2))) float f32x2;
typedef __attribute__((ext_vector_type(4))) float f32x4;
typedef __attribute__((ext_vector_type(16))) float f32x16;
typedef __attribute__((ext_vector_type(4))) unsigned u32x4;
typedef __attribute__((ext_vector_type(2))) unsigned u32x2;
typedef __amdgpu_buffer_rsrc_t rsrc_t;

constexpr int NB = 8, SQ = 32, DM = 512, NH = 32, HKV = 8, SK = 2048, DH = 128;
constexpr int NT = 32;
constexpr unsigned SCR = 131072;
constexpr unsigned LDS_BYTES = 163840;
constexpr float SC2 = 0.08838834764831845f * 1.4426950408889634f;
constexpr unsigned WS_GEN = 0, WS_SEEN = 1024, WS_FLAG = 4096, FS = 32, WS_O = 262144, WS_BOX = WS_O + (4u << 20);
constexpr unsigned SPIN_CAP = 1u << 17;
constexpr int QBUF = 3;
constexpr float LAZY = 6.f;

DI unsigned rfl(unsigned x) { return __builtin_amdgcn_readfirstlane(x); }
DI rsrc_t mk_rsrc(const void* p, unsigned bytes) { return __builtin_amdgcn_make_buffer_rsrc((void*)p, 0, bytes, 0x00020000); }
DI unsigned cvtpk(float lo, float hi) { f32x2 v = {lo, hi}; bf16x2 b = __builtin_convertvector(v, bf16x2); return __builtin_bit_cast(unsigned, b); }
DI void bar() { asm volatile("s_waitcnt lgkmcnt(0)\n\ts_barrier" ::: "memory"); }
template <int N> DI void vmwait() { asm volatile("s_waitcnt vmcnt(%0)" :: "n"(N) : "memory"); }
#define SBAR() __builtin_amdgcn_sched_barrier(0)

#define TILE8(NTS) \
    "buffer_load_dwordx4 %6, %5, %3 offen" NTS " lds\n\t" \
    "buffer_load_dwordx4 %7, %5, %3 offen offset:1024" NTS " lds\n\t" \
    "buffer_load_dwordx4 %8, %5, %3 offen offset:2048" NTS " lds\n\t" \
    "buffer_load_dwordx4 %9, %5, %3 offen offset:3072" NTS " lds\n\t" \
    "s_mov_b32 m0, %2\n\ts_nop 0\n\t" \
    "buffer_load_dwordx4 %6, %5, %4 offen" NTS " lds\n\t" \
    "buffer_load_dwordx4 %7, %5, %4 offen offset:1024" NTS " lds\n\t" \
    "buffer_load_dwordx4 %8, %5, %4 offen offset:2048" NTS " lds\n\t" \
    "buffer_load_dwordx4 %9, %5, %4 offen offset:3072" NTS " lds\n\t"
DI void dma_tile(unsigned nt, unsigned lds, rsrc_t srd, unsigned soff, const unsigned (&vo)[4]) {
  asm volatile("s_mov_b32 m0, %1\n\ts_cmp_eq_u32 %0, 0\n\ts_waitcnt lgkmcnt(0)\n\ts_cbranch_scc1 .Ldef%=\n\t" TILE8(" nt") "s_branch .Lend%=\n.Ldef%=:\n\t" TILE8("") ".Lend%=:"
               :: "s"(nt), "s"(lds), "s"(lds + 4096u), "s"(soff), "s"(soff + 4096u), "s"(srd), "v"(vo[0]), "v"(vo[1]), "v"(vo[2]), "v"(vo[3]) : "memory", "scc");
}
DI void dma_piece(unsigned lds, rsrc_t srd, unsigned voff, unsigned soff) {
  asm volatile("s_mov_b32 m0, %0\n\ts_nop 0\n\tbuffer_load_dwordx4 %1, %2, %3 offen lds" :: "s"(lds), "v"(voff), "s"(srd), "s"(soff) : "memory");
}
DI u32x4 bload(rsrc_t srd, unsigned voff, unsigned soff) {
  u32x4 r; asm volatile("buffer_load_dwordx4 %0, %1, %2, %3 offen" : "=v"(r) : "v"(voff), "s"(srd), "s"(soff) : "memory"); return r;
}
DI u32x4 bload_sc1(rsrc_t srd, unsigned voff, unsigned soff) {
  u32x4 r; asm volatile("buffer_load_dwordx4 %0, %1, %2, %3 offen sc1" : "=v"(r) : "v"(voff), "s"(srd), "s"(soff) : "memory"); return r;
}

struct Att {
  unsigned ldsK, nt, kh; rsrc_t srdK, srdV; unsigned vo[4], ka[8], va[8];
  bf16x8 qf[8]; f32x16 o[4]; float m, l;
};

template <bool MORE> DI void take_k(Att& a, int t, bf16x8 (&kf)[8]) {
  vmwait<8>();
#pragma unroll
  for (int s = 0; s < 8; ++s) kf[s] = *(LAS bf16x8*)(uintptr_t)a.ka[s];
  SBAR();
  if (MORE) dma_tile(a.nt, a.ldsK, a.srdK, (unsigned)((t + 1) * 2 + a.kh) * 8192u, a.vo);
  SBAR();
}
template <bool MORE> DI void take_v(Att& a, int t, bf16x8 (&vf)[4][2]) {
  if (MORE) vmwait<8>(); else vmwait<0>();
#pragma unroll
  for (int cb = 0; cb < 4; ++cb)
#pragma unroll
    for (int s = 0; s < 2; ++s) {
      const bf16x4 lo = __builtin_amdgcn_ds_read_tr16_b64_v4bf16((LAS bf16x4*)(uintptr_t)a.va[2 * cb] + 512 * s);
      const bf16x4 up = __builtin_amdgcn_ds_read_tr16_b64_v4bf16((LAS bf16x4*)(uintptr_t)a.va[2 * cb + 1] + 512 * s + 256);
      vf[cb][s] = __builtin_shufflevector(lo, up, 0, 1, 2, 3, 4, 5, 6, 7);
    }
  SBAR();
  if (MORE) dma_tile(a.nt, a.ldsK + 8192u, a.srdV, (unsigned)((t + 1) * 2 + a.kh) * 8192u, a.vo);
  SBAR();
}
template <bool FIRST, bool MORE> DI void attn_tile(Att& a, int t, bf16x8 (&kf)[8], bf16x8 (&vf)[4][2]) {
  if (!FIRST) take_k<MORE>(a, t, kf);
  f32x16 sc = {};
#pragma unroll
  for (int s = 0; s < 8; ++s) sc = __builtin_amdgcn_mfma_f32_32x32x16_bf16(kf[s], a.qf[s], sc, 0, 0, 0);
  float mx = sc[0];
#pragma unroll
  for (int i = 1; i < 16; ++i) mx = fmaxf(mx, sc[i]);
  { auto rr = __builtin_amdgcn_permlane32_swap(__float_as_uint(mx), __float_as_uint(mx), false, false); mx = fmaxf(__uint_as_float(rr[0]), __uint_as_float(rr[1])); }
  if (FIRST) a.m = mx;
  else if (__builtin_expect(__any((mx - a.m) * SC2 > LAZY), 0)) {
    const float mn = fmaxf(a.m, mx), alpha = __builtin_amdgcn_exp2f((a.m - mn) * SC2);
    a.m = mn; a.l *= alpha;
#pragma unroll
    for (int cb = 0; cb < 4; ++cb) a.o[cb] *= alpha;
  }
  const float nm = -a.m * SC2;
  float ps = 0.f;
#pragma unroll
  for (int i = 0; i < 16; ++i) { sc[i] = __builtin_amdgcn_exp2f(fmaf(sc[i], SC2, nm)); ps += sc[i]; }
  a.l = FIRST ? ps : a.l + ps;
  bf16x8 pf[2];
#pragma unroll
  for (int s = 0; s < 2; ++s)
    pf[s] = __builtin_bit_cast(bf16x8, u32x4{cvtpk(sc[8 * s], sc[8 * s + 1]), cvtpk(sc[8 * s + 2], sc[8 * s + 3]), cvtpk(sc[8 * s + 4], sc[8 * s + 5]), cvtpk(sc[8 * s + 6], sc[8 * s + 7])});
  asm volatile("" : "+v"(pf[0]), "+v"(pf[1]), "+v"(a.l));
  if (!FIRST) take_v<MORE>(a, t, vf);
#pragma unroll
  for (int s = 0; s < 2; ++s)
#pragma unroll
    for (int cb = 0; cb < 4; ++cb) a.o[cb] = __builtin_amdgcn_mfma_f32_32x32x16_bf16(vf[cb][s], pf[s], FIRST && s == 0 ? f32x16{} : a.o[cb], 0, 0, 0);
}

__global__ __launch_bounds__(256, 2) void fused_kernel(const void* __restrict__ xA, const void* __restrict__ wqA, const void* __restrict__ kA, const void* __restrict__ vA,
                                                       const void* __restrict__ xB, const void* __restrict__ wqB, const void* __restrict__ kB, const void* __restrict__ vB,
                                                       const void* __restrict__ woA, const void* __restrict__ woB, char* ws, __hip_bfloat16* __restrict__ out) {
  extern __shared__ __attribute__((aligned(16))) char smem_[];
  LAS char* smem = (LAS char*)smem_;
  const unsigned lds0 = (unsigned)(uintptr_t)smem;
  const int lane = threadIdx.x & 63, r32 = lane & 31, hi = lane >> 5;
  const int wave = rfl(threadIdx.x >> 6), br = wave >> 1, kh = wave & 1;
  const unsigned ring = wave * 32768u;
  const int g8 = blockIdx.x & 7, jj = blockIdx.x >> 3, b = jj >> 2, head = 4 * g8 + (jj & 3);
  Att a;
  bf16x8 kf0[8], vf0[4][2];
  {
    const int cc = lane & 15, g = lane >> 4;
    unsigned wvo = g * 65536 + 16 * cc, xvo = hi * 1024 + 16 * (r32 ^ hi);
    const unsigned ximg = ring + 16384;
    asm volatile("" : "+v"(wvo), "+v"(xvo));
    SBAR();
    const rsrc_t srdX = mk_rsrc((const char*)(br ? xB : xA) + (size_t)b * (SQ * DM * 2), SQ * DM * 2);
    const rsrc_t srdW = mk_rsrc(br ? wqB : wqA, DM * NH * DH * 2);
    u32x4 wr[8][8];
    f32x4 acc[8][2];
#define XISSUE(I0, I1) _Pragma("unroll") for (int i = (I0); i < (I1); ++i) { const unsigned p = (unsigned)(i + 4 * jj) & 15u; \
      dma_piece(lds0 + ximg + 1024u * p, srdX, xvo ^ (32u * (p & 7u)), p * 2048u + kh * 512u); }
#define QISSUE(ks) do { const unsigned kk = (unsigned)(kh * 8 + (((ks) + b) & 7)); \
      _Pragma("unroll") for (int i = 0; i < 8; ++i) wr[ks][i] = bload(srdW, wvo, (kk * 32 + i) * 8192 + head * 256); } while (0)
    XISSUE(0, 4); QISSUE(0); XISSUE(4, 16);
#pragma unroll
    for (int ks = 1; ks < QBUF; ++ks) QISSUE(ks);
    {
      a.ldsK = lds0 + ring; a.nt = br; a.kh = kh;
      const size_t kvoff = (size_t)(br ? b * NH + head : b * HKV + g8) * (SK * DH * 2);
      a.srdK = mk_rsrc((const char*)(br ? kB : kA) + kvoff, SK * DH * 2);
      a.srdV = mk_rsrc((const char*)(br ? vB : vA) + kvoff, SK * DH * 2);
#pragma unroll
      for (int i = 0; i < 4; ++i) a.vo[i] = g * 256 + 16 * (cc ^ ((g << 2) | i));
      asm volatile("" :: "s"(woA), "s"(woB), "s"(ws), "s"(out));
      const unsigned kaddr = ring + 256 * r32 + 16 * (hi ^ (((r32 & 3) << 2) | ((r32 >> 2) & 3)));
      const int blk = (lane >> 4) & 1, q4 = (lane & 15) >> 2, p4 = lane & 3;
      const unsigned vaddr = (ring + 8192 + 256 * (4 * hi + q4) + 64 * q4 + 16 * ((2 * blk + (p4 & 1)) ^ hi) + 8 * (p4 >> 1)) ^ (kh * 128u);
#pragma unroll
      for (int s = 0; s < 8; ++s) { a.ka[s] = lds0 + (kaddr ^ (32u * s)); a.va[s] = lds0 + (vaddr ^ (32u * s)); }
      asm volatile("" : "+v"(a.ka[0]), "+v"(a.ka[1]), "+v"(a.ka[2]), "+v"(a.ka[3]), "+v"(a.ka[4]), "+v"(a.ka[5]), "+v"(a.ka[6]), "+v"(a.ka[7]),
                        "+v"(a.va[0]), "+v"(a.va[1]), "+v"(a.va[2]), "+v"(a.va[3]), "+v"(a.va[4]), "+v"(a.va[5]), "+v"(a.va[6]), "+v"(a.va[7]));
    }
#pragma unroll
    for (int ks = 0; ks < 8; ++ks) {
      SBAR();
      const int n = ((ks + QBUF - 1 < 7 ? ks + QBUF - 1 : 7) - ks) * 8 + (ks > 5 ? 8 : 0);
      asm volatile("s_waitcnt vmcnt(%8)" : "+v"(wr[ks][0]), "+v"(wr[ks][1]), "+v"(wr[ks][2]), "+v"(wr[ks][3]), "+v"(wr[ks][4]), "+v"(wr[ks][5]), "+v"(wr[ks][6]), "+v"(wr[ks][7]) : "n"(n) : "memory");
      if (ks + QBUF < 8) QISSUE((ks + QBUF) & 7);
      SBAR();
      bf16x8 xf[2];
      const unsigned ch = 4u * ((ks + b) & 7) + g;
#pragma unroll
      for (int qb = 0; qb < 2; ++qb) xf[qb] = *(LAS bf16x8*)(smem + ximg + (cc + 16 * (qb ^ kh)) * 512 + 16 * (ch ^ cc));
#pragma unroll
      for (int j = 0; j < 8; ++j) {
        u32x4 f;
#pragma unroll
        for (int m = 0; m < 4; ++m) f[m] = __builtin_amdgcn_perm(wr[ks][2 * m + 1][j >> 1], wr[ks][2 * m][j >> 1], (j & 1) ? 0x07060302u : 0x05040100u);
#pragma unroll
        for (int qb = 0; qb < 2; ++qb)
          acc[j][qb] = __builtin_amdgcn_mfma_f32_16x16x32_bf16(__builtin_bit_cast(bf16x8, f), xf[qb], ks ? acc[j][qb] : f32x4{0.f, 0.f, 0.f, 0.f}, 0, 0, 0);
      }
      __builtin_amdgcn_sched_group_barrier(0x100, 2, 0);
      __builtin_amdgcn_sched_group_barrier(0x2, 8, 0);
#pragma unroll
      for (int j = 0; j < 12; ++j) { __builtin_amdgcn_sched_group_barrier(0x8, 1, 0); __builtin_amdgcn_sched_group_barrier(0x2, 2, 0); }
      __builtin_amdgcn_sched_group_barrier(0x8, 4, 0);
      SBAR();
      if (ks == 5) dma_tile(a.nt, a.ldsK, a.srdK, kh * 8192u, a.vo);
      if (ks == 7) dma_tile(a.nt, a.ldsK + 8192u, a.srdV, kh * 8192u, a.vo);
    }
#undef XISSUE
#undef QISSUE
    LAS f32x4* mine = (LAS f32x4*)(smem + SCR + wave * 8192) + lane;
#pragma unroll
    for (int j = 0; j < 8; ++j) mine[j * 64] = acc[j][1];
    take_k<true>(a, 0, kf0);
    bar();
    LAS f32x4* other = (LAS f32x4*)(smem + SCR + (wave ^ 1) * 8192) + lane;
#pragma unroll
    for (int j = 0; j < 8; ++j) acc[j][0] += other[j * 64];
    LAS char* qimg = smem + (wave & 2) * 32768 + 16384;
#pragma unroll
    for (int e = 0; e < 4; ++e)
      *(LAS u32x4*)(qimg + (cc + 16 * kh) * 256 + 16 * ((4 * g + e) ^ cc)) =
          u32x4{cvtpk(acc[0][0][e], acc[1][0][e]), cvtpk(acc[2][0][e], acc[3][0][e]), cvtpk(acc[4][0][e], acc[5][0][e]), cvtpk(acc[6][0][e], acc[7][0][e])};
    take_v<true>(a, 0, vf0);
    bar();
#pragma unroll
    for (int s = 0; s < 8; ++s) a.qf[s] = *(LAS bf16x8*)(qimg + r32 * 256 + 16 * ((2 * s + hi) ^ (lane & 15)));
  }

  attn_tile<true, true>(a, 0, kf0, vf0);
#pragma unroll 1
  for (int t = 1; t < NT - 1; ++t) { bf16x8 kf[8], vf[4][2]; attn_tile<false, true>(a, t, kf, vf); }
  { bf16x8 kf[8], vf[4][2]; attn_tile<false, false>(a, NT - 1, kf, vf); }

  unsigned long long gen = __hip_atomic_load((GAS unsigned long long*)(ws + WS_GEN), RLX_AGENT);
  GAS unsigned* flags = (GAS unsigned*)(ws + WS_FLAG);

  {
    { auto rr = __builtin_amdgcn_permlane32_swap(__float_as_uint(a.l), __float_as_uint(a.l), false, false); a.l = __uint_as_float(rr[0]) + __uint_as_float(rr[1]); }
    LAS f32x4* mine = (LAS f32x4*)(smem + SCR + wave * 8192) + lane;
    LAS f32x4* other = (LAS f32x4*)(smem + SCR + (wave ^ 1) * 8192) + lane;
    *(LAS f32x2*)(smem + ring + lane * 8) = f32x2{a.m, a.l};
#pragma unroll
    for (int u = 0; u < 8; ++u) mine[u * 64] = f32x4{a.o[2 + (u >> 2)][4 * (u & 3)], a.o[2 + (u >> 2)][4 * (u & 3) + 1], a.o[2 + (u >> 2)][4 * (u & 3) + 2], a.o[2 + (u >> 2)][4 * (u & 3) + 3]};
    const rsrc_t srdOw = mk_rsrc(ws + WS_O + (size_t)((br * NB + b) * NH + head) * 8192 + kh * 4096, 4096);
    bar();
    const f32x2 v = *(LAS f32x2*)(smem + (ring ^ 32768u) + lane * 8);
    f32x4 p[8];
#pragma unroll
    for (int u = 0; u < 8; ++u) p[u] = other[u * 64];
    SBAR();
    const float ms = fmaxf(a.m, v[0]), e0 = __builtin_amdgcn_exp2f((a.m - ms) * SC2), e1 = __builtin_amdgcn_exp2f((v[0] - ms) * SC2);
    const float inv = __builtin_amdgcn_rcpf(fmaf(e0, a.l, e1 * v[1])), f0 = e0 * inv, f1 = e1 * inv;
#pragma unroll
    for (int u = 0; u < 4; ++u) {
      const f32x4 a0 = p[2 * u] * f1, a1 = p[2 * u + 1] * f1;
      const int cb = u >> 1, r0 = 8 * (u & 1);
      const u32x4 w = {cvtpk(fmaf(a.o[cb][r0], f0, a0[0]), fmaf(a.o[cb][r0 + 1], f0, a0[1])), cvtpk(fmaf(a.o[cb][r0 + 2], f0, a0[2]), fmaf(a.o[cb][r0 + 3], f0, a0[3])),
                       cvtpk(fmaf(a.o[cb][r0 + 4], f0, a1[0]), fmaf(a.o[cb][r0 + 5], f0, a1[1])), cvtpk(fmaf(a.o[cb][r0 + 6], f0, a1[2]), fmaf(a.o[cb][r0 + 7], f0, a1[3]))};
      __builtin_amdgcn_raw_buffer_store_b128(w, srdOw, lane * 16 + u * 1024, 0, 16);
    }
  }
  SBAR();
  const int obr = g8 >> 2, okq = g8 & 3, ob = 2 * (jj >> 3), ocb = jj & 7, h0 = okq * 8 + ((wave + ocb + (ob >> 1)) & 3) * 2, orot = (ob + ocb) & 7;
  const rsrc_t srdWo = mk_rsrc(obr ? woB : woA, NH * DH * DM * 2);
  const rsrc_t srdO = mk_rsrc(ws + WS_O + (size_t)((obr * NB + ob) * NH + h0) * 8192, NH * 8192 + 2 * 8192);
  unsigned wvo = (lane >> 3) * 1024 + 16 * ((lane & 7) ^ (((lane >> 4) & 1) << 2));
  unsigned waddr = (ring + 128 * (8 * hi + ((lane & 15) >> 2)) + 64 * ((lane & 15) >> 3) + 32 * ((lane >> 4) & 1) + 8 * (lane & 3)) ^ (16u * wave) ^ (64u * (okq & 1));
  GAS unsigned* fp = flags + ((obr * NB + ob + ((lane >> 1) & 1)) * NH + h0 + (lane & 1)) * FS;
  GAS unsigned* myflag = threadIdx.x < 2 ? flags + ((threadIdx.x * NB + b) * NH + head) * FS : (GAS unsigned*)(ws + WS_SEEN) + blockIdx.x;
  asm volatile("" : "+v"(wvo), "+v"(waddr), "+v"(fp), "+v"(myflag) : "s"(srdWo), "s"(srdO));
  vmwait<0>();
  asm volatile("" : "+v"(gen));
  const unsigned long long tag64 = (((unsigned long long)rfl((unsigned)(gen >> 32)) << 32) | rfl((unsigned)gen)) + 1ull;
  const unsigned tag = (unsigned)tag64;
  bar();
  if (threadIdx.x < 3) __hip_atomic_store(myflag, tag, RLX_AGENT);
  f32x16 acc[4] = {};
  {
#define DMA_W(I0, I1) _Pragma("unroll") for (int i = (I0); i < (I1); ++i)        \
      dma_piece(lds0 + ring + 1024u * i, srdWo, wvo, (unsigned)(h0 * 128 + (((i >> 2) + orot) & 7) * 32 + (i & 3) * 8) * 1024u + ocb * 128u)
#define RD_W(S0, S1) _Pragma("unroll") for (int s = (S0); s < (S1); ++s) _Pragma("unroll") for (int nb = 0; nb < 2; ++nb) { \
      const bf16x4 lo = __builtin_amdgcn_ds_read_tr16_b64_v4bf16((LAS bf16x4*)(smem + (waddr ^ (64u * nb)) + 2048u * s)); \
      const bf16x4 up = __builtin_amdgcn_ds_read_tr16_b64_v4bf16((LAS bf16x4*)(smem + (waddr ^ (64u * nb)) + 2048u * s + 512u)); \
      wf[s][nb] = __builtin_shufflevector(lo, up, 0, 1, 2, 3, 4, 5, 6, 7); }
#define O_ISSUE(S0, S1) _Pragma("unroll") for (int s = (S0); s < (S1); ++s) _Pragma("unroll") for (int bi = 0; bi < 2; ++bi) \
      of[s][bi] = bload_sc1(srdO, lane * 16, (unsigned)((s + 2 * orot) & 15) * 1024u + (bi ^ (okq >> 1)) * (NH * 8192u))
#define O_WAIT(S, N) asm volatile("s_waitcnt vmcnt(%4)" : "+v"(of[S][0]), "+v"(of[S][1]), "+v"(of[(S) + 1][0]), "+v"(of[(S) + 1][1]) : "n"(N) : "memory")
#define O_MFMA(S0) _Pragma("unroll") for (int s = (S0); s < (S0) + 2; ++s) _Pragma("unroll") for (int j = 0; j < 4; ++j) \
      acc[j] = __builtin_amdgcn_mfma_f32_32x32x16_bf16(wf[s][j & 1], __builtin_bit_cast(bf16x8, of[s][j >> 1]), acc[j], 0, 0, 0)
    unsigned f0;
    asm volatile("global_load_dword %0, %1, off sc1" : "=v"(f0) : "v"(fp) : "memory");
    DMA_W(0, 8);
    asm volatile("s_waitcnt vmcnt(8)" : "+v"(f0) :: "memory");
    if (__all(f0 == tag)) {
      u32x4 of[16][2]; bf16x8 wf[16][2];
      O_ISSUE(0, 4); DMA_W(8, 16);
#pragma unroll
      for (int s0 = 0; s0 < 16; s0 += 2) {
        SBAR();
        O_WAIT(s0, s0 == 2 ? 16 : s0 <= 8 ? 12 : 28 - 2 * s0);
        RD_W(s0, s0 + 2);
        if (s0 + 4 < 16) O_ISSUE(s0 + 4, s0 + 6);
        if (s0 < 8) DMA_W(2 * s0 + 16, 2 * s0 + 20);
        SBAR();
        O_MFMA(s0);
      }
    } else {
      unsigned f1;
      DMA_W(8, 24);
      asm volatile("global_load_dword %0, %1, off sc1" : "=v"(f1) : "v"(fp) : "memory");
      DMA_W(24, 32);
      asm volatile("s_waitcnt vmcnt(%1)" : "+v"(f1) : "n"(8) : "memory");
      if (!__all(f1 == tag)) {
#pragma unroll 1
        for (unsigned spins = 0; spins < SPIN_CAP; ++spins) {
          if (__all(__hip_atomic_load(fp, RLX_AGENT) == tag)) break;
          __builtin_amdgcn_s_sleep(4);
        }
      }
      asm volatile("" ::: "memory");
      {
        u32x4 of[16][2]; bf16x8 wf[16][2];
        O_ISSUE(0, 12);
        vmwait<24>();
        RD_W(0, 4);
#pragma unroll
        for (int s0 = 0; s0 < 16; s0 += 2) {
          SBAR();
          O_WAIT(s0, s0 < 4 ? 20 : 28 - 2 * s0);
          if (s0 < 4) O_ISSUE(12 + s0, 14 + s0);
          if (s0 + 4 < 16) RD_W(s0 + 4, s0 + 6);
          SBAR();
          O_MFMA(s0);
        }
      }
    }
#undef DMA_W
#undef RD_W
#undef O_ISSUE
#undef O_WAIT
#undef O_MFMA
  }
#pragma unroll
  for (int u = 0; u < 16; ++u)
    if (u & 3) *((LAS f32x4*)(smem + ring) + u * 64 + lane) = f32x4{acc[u >> 2][4 * (u & 3)], acc[u >> 2][4 * (u & 3) + 1], acc[u >> 2][4 * (u & 3) + 2], acc[u >> 2][4 * (u & 3) + 3]};
  {
    const unsigned t0 = tag & 0x3fffffu, t1 = ((unsigned)(tag64 >> 22) ^ tag) & 0x3fffffu;
    const rsrc_t srdB = mk_rsrc(ws + WS_BOX, 256 * 16384), srdOut = mk_rsrc(out, NB * SQ * 2048);
    unsigned bvo = blockIdx.x * 16384 + threadIdx.x * 16, ovo = r32 * 2048 + 8 * hi, ra[3];
    const unsigned oso = (ob + (okq >> 1)) * SQ * 2048 + obr * 1024 + ocb * 128 + (okq & 1) * 64 + 16 * wave;
#pragma unroll
    for (int x = 1; x < 4; ++x) ra[x - 1] = lds0 + (ring ^ (x * 32768u)) + x * 1024 + lane * 16;
    asm volatile("" : "+v"(bvo), "+v"(ovo), "+v"(ra[0]), "+v"(ra[1]), "+v"(ra[2]) : "s"(srdB), "s"(srdOut), "s"(oso), "s"(t1));
    bar();
    f32x4 p[4][3], s;
#pragma unroll
    for (int r = 3; r >= 0; --r)
#pragma unroll
      for (int x = 0; x < 3; ++x) p[r][x] = *((const LAS f32x4*)(uintptr_t)ra[x] + r * 256);
    SBAR();
#pragma unroll
    for (int r = 3; r >= 0; --r) {
      s = f32x4{acc[r][0], acc[r][1], acc[r][2], acc[r][3]} + p[r][0] + p[r][1] + p[r][2];
      if (r) {
        const u32x4 u = __builtin_bit_cast(u32x4, s) + 0x400u;
        const u32x4 w = {__builtin_amdgcn_alignbit(u[1] >> 11, u[0], 11), __builtin_amdgcn_alignbit(t0, u[1], 22), __builtin_amdgcn_alignbit(u[3] >> 11, u[2], 11), __builtin_amdgcn_alignbit(t1, u[3], 22)};
        __builtin_amdgcn_raw_buffer_store_b128(w, srdB, bvo ^ (r * 20480u), 0, 16);
      }
    }
    u32x4 g[3];
#pragma unroll
    for (int i = 0; i < 3; ++i) g[i] = __builtin_amdgcn_raw_buffer_load_b128(srdB, bvo, (1 + i) * 4096, 16);
    asm volatile("" : "+v"(s));
#pragma unroll 1
    for (unsigned spins = 0; spins < SPIN_CAP; ++spins) {
      unsigned bad = 0;
#pragma unroll
      for (int i = 0; i < 3; ++i) bad |= (g[i][1] ^ (t0 << 10)) | (g[i][3] ^ (t1 << 10));
      if (__all(bad < 1024u)) break;
      asm volatile("s_sleep 1" ::: "memory");
#pragma unroll
      for (int i = 0; i < 3; ++i) g[i] = __builtin_amdgcn_raw_buffer_load_b128(srdB, bvo, (1 + i) * 4096, 16);
    }
#pragma unroll
    for (int i = 0; i < 3; ++i)
      s += __builtin_bit_cast(f32x4, u32x4{g[i][0], __builtin_amdgcn_alignbit(g[i][1], g[i][0], 21), g[i][2], __builtin_amdgcn_alignbit(g[i][3], g[i][2], 21)} << 11);
    __builtin_amdgcn_raw_buffer_store_b64(u32x2{cvtpk(s[0], s[1]), cvtpk(s[2], s[3])}, srdOut, ovo, oso, 16);
  }
  if (blockIdx.x == 0 && wave == 0) {
    const rsrc_t srdS = mk_rsrc(ws + WS_SEEN, 1024);
#pragma unroll 1
    for (unsigned spins = 0; spins < SPIN_CAP; ++spins) {
      const u32x4 v = __builtin_amdgcn_raw_buffer_load_b128(srdS, lane * 16, 0, 16);
      if (__all(((v[0] ^ tag) | (v[1] ^ tag) | (v[2] ^ tag) | (v[3] ^ tag)) == 0)) break;
      asm volatile("s_sleep 1" ::: "memory");
    }
    if (lane == 0) __hip_atomic_store((GAS unsigned long long*)(ws + WS_GEN), tag64, RLX_AGENT);
  }
}

extern "C" void kernel_launch(void* const* d_in, const int* in_sizes, int n_in, void* d_out, int out_size, void* d_ws, size_t ws_size, hipStream_t stream) {
  (void)in_sizes; (void)n_in; (void)out_size; (void)ws_size;
  (void)hipFuncSetAttribute((const void*)fused_kernel, hipFuncAttributeMaxDynamicSharedMemorySize, LDS_BYTES);
  hipLaunchKernelGGL(fused_kernel, dim3(256), dim3(256), LDS_BYTES, stream, d_in[0], d_in[3], d_in[1], d_in[2], d_in[5], d_in[8], d_in[6], d_in[7], d_in[4], d_in[9],
                     (char*)d_ws, (__hip_bfloat16*)d_out);
}
```

```cpp
#include <hip/hip_runtime.h>
#include <hip/hip_bf16.h>
#include <stdint.h>

#define DI __device__ __forceinline__
#define LAS __attribute__((address_space(3)))
#define GAS __attribute__((address_space(1)))
#define RLX_AGENT __ATOMIC_RELAXED, __HIP_MEMORY_SCOPE_AGENT

typedef __attribute__((ext_vector_type(8))) __bf16 bf16x8;
typedef __attribute__((ext_vector_type(4))) __bf16 bf16x4;
typedef __attribute__((ext_vector_type(2))) __bf16 bf16x2;
typedef __attribute__((ext_vector_type(2))) float f32x2;
typedef __attribute__((ext_vector_type(4))) float f32x4;
typedef __attribute__((ext_vector_type(16))) float f32x16;
typedef __attribute__((ext_vector_type(4))) unsigned u32x4;
typedef __attribute__((ext_vector_type(2))) unsigned u32x2;
typedef __amdgpu_buffer_rsrc_t rsrc_t;

constexpr int NB = 8, SQ = 32, DM = 512, NH = 32, HKV = 8, SK = 2048, DH = 128;
constexpr int NT = 32;
constexpr unsigned SCR = 131072;
constexpr unsigned LDS_BYTES = 163840;
constexpr float SC2 = 0.08838834764831845f * 1.4426950408889634f;
constexpr unsigned WS_GEN = 0, WS_SEEN = 1024, WS_FLAG = 4096, FS = 32, WS_O = 262144, WS_BOX = WS_O + (4u << 20);
constexpr unsigned SPIN_CAP = 1u << 17;
constexpr int QBUF = 3;
constexpr float LAZY = 6.f;

DI unsigned rfl(unsigned x) { return __builtin_amdgcn_readfirstlane(x); }
DI rsrc_t mk_rsrc(const void* p, unsigned bytes) { return __builtin_amdgcn_make_buffer_rsrc((void*)p, 0, bytes, 0x00020000); }
DI unsigned cvtpk(float lo, float hi) { f32x2 v = {lo, hi}; bf16x2 b = __builtin_convertvector(v, bf16x2); return __builtin_bit_cast(unsigned, b); }
DI void bar() { asm volatile("s_waitcnt lgkmcnt(0)\n\ts_barrier" ::: "memory"); }
template <int N> DI void vmwait() { asm volatile("s_waitcnt vmcnt(%0)" :: "n"(N) : "memory"); }
#define SBAR() __builtin_amdgcn_sched_barrier(0)

#define TILE8(NTS) \
    "buffer_load_dwordx4 %6, %5, %3 offen" NTS " lds\n\t" \
    "buffer_load_dwordx4 %7, %5, %3 offen offset:1024" NTS " lds\n\t" \
    "buffer_load_dwordx4 %8, %5, %3 offen offset:2048" NTS " lds\n\t" \
    "buffer_load_dwordx4 %9, %5, %3 offen offset:3072" NTS " lds\n\t" \
    "s_mov_b32 m0, %2\n\ts_nop 0\n\t" \
    "buffer_load_dwordx4 %6, %5, %4 offen" NTS " lds\n\t" \
    "buffer_load_dwordx4 %7, %5, %4 offen offset:1024" NTS " lds\n\t" \
    "buffer_load_dwordx4 %8, %5, %4 offen offset:2048" NTS " lds\n\t" \
    "buffer_load_dwordx4 %9, %5, %4 offen offset:3072" NTS " lds\n\t"
DI void dma_tile(unsigned nt, unsigned lds, rsrc_t srd, unsigned soff, const unsigned (&vo)[4]) {
  asm volatile("s_mov_b32 m0, %1\n\ts_cmp_eq_u32 %0, 0\n\ts_waitcnt lgkmcnt(0)\n\ts_cbranch_scc1 .Ldef%=\n\t" TILE8(" nt") "s_branch .Lend%=\n.Ldef%=:\n\t" TILE8("") ".Lend%=:"
               :: "s"(nt), "s"(lds), "s"(lds + 4096u), "s"(soff), "s"(soff + 4096u), "s"(srd), "v"(vo[0]), "v"(vo[1]), "v"(vo[2]), "v"(vo[3]) : "memory", "scc");
}
DI void dma_piece(unsigned lds, rsrc_t srd, unsigned voff, unsigned soff) {
  asm volatile("s_mov_b32 m0, %0\n\ts_nop 0\n\tbuffer_load_dwordx4 %1, %2, %3 offen lds" :: "s"(lds), "v"(voff), "s"(srd), "s"(soff) : "memory");
}
DI u32x4 bload(rsrc_t srd, unsigned voff, unsigned soff) {
  u32x4 r; asm volatile("buffer_load_dwordx4 %0, %1, %2, %3 offen" : "=v"(r) : "v"(voff), "s"(srd), "s"(soff) : "memory"); return r;
}
DI u32x4 bload_sc1(rsrc_t srd, unsigned voff, unsigned soff) {
  u32x4 r; asm volatile("buffer_load_dwordx4 %0, %1, %2, %3 offen sc1" : "=v"(r) : "v"(voff), "s"(srd), "s"(soff) : "memory"); return r;
}

struct Att {
  unsigned ldsK, nt, kh; rsrc_t srdK, srdV; unsigned vo[4], ka[8], va[8];
  bf16x8 qf[8]; f32x16 o[4]; float m, l;
};

template <bool MORE> DI void take_k(Att& a, int t, bf16x8 (&kf)[8]) {
  vmwait<8>();
#pragma unroll
  for (int s = 0; s < 8; ++s) kf[s] = *(LAS bf16x8*)(uintptr_t)a.ka[s];
  SBAR();
  if (MORE) dma_tile(a.nt, a.ldsK, a.srdK, (unsigned)((t + 1) * 2 + a.kh) * 8192u, a.vo);
  SBAR();
}
template <bool MORE> DI void take_v(Att& a, int t, bf16x8 (&vf)[4][2]) {
  if (MORE) vmwait<8>(); else vmwait<0>();
#pragma unroll
  for (int cb = 0; cb < 4; ++cb)
#pragma unroll
    for (int s = 0; s < 2; ++s) {
      const bf16x4 lo = __builtin_amdgcn_ds_read_tr16_b64_v4bf16((LAS bf16x4*)(uintptr_t)a.va[2 * cb] + 512 * s);
      const bf16x4 up = __builtin_amdgcn_ds_read_tr16_b64_v4bf16((LAS bf16x4*)(uintptr_t)a.va[2 * cb + 1] + 512 * s + 256);
      vf[cb][s] = __builtin_shufflevector(lo, up, 0, 1, 2, 3, 4, 5, 6, 7);
    }
  SBAR();
  if (MORE) dma_tile(a.nt, a.ldsK + 8192u, a.srdV, (unsigned)((t + 1) * 2 + a.kh) * 8192u, a.vo);
  SBAR();
}
template <bool FIRST, bool MORE> DI void attn_tile(Att& a, int t, bf16x8 (&kf)[8], bf16x8 (&vf)[4][2]) {
  if (!FIRST) take_k<MORE>(a, t, kf);
  f32x16 sc = {};
#pragma unroll
  for (int s = 0; s < 8; ++s) sc = __builtin_amdgcn_mfma_f32_32x32x16_bf16(kf[s], a.qf[s], sc, 0, 0, 0);
  float mx = sc[0];
#pragma unroll
  for (int i = 1; i < 16; ++i) mx = fmaxf(mx, sc[i]);
  { auto rr = __builtin_amdgcn_permlane32_swap(__float_as_uint(mx), __float_as_uint(mx), false, false); mx = fmaxf(__uint_as_float(rr[0]), __uint_as_float(rr[1])); }
  if (FIRST) a.m = mx;
  else if (__builtin_expect(__any((mx - a.m) * SC2 > LAZY), 0)) {
    const float mn = fmaxf(a.m, mx), alpha = __builtin_amdgcn_exp2f((a.m - mn) * SC2);
    a.m = mn; a.l *= alpha;
#pragma unroll
    for (int cb = 0; cb < 4; ++cb) a.o[cb] *= alpha;
  }
  const float nm = -a.m * SC2;
  float ps = 0.f;
#pragma unroll
  for (int i = 0; i < 16; ++i) { sc[i] = __builtin_amdgcn_exp2f(fmaf(sc[i], SC2, nm)); ps += sc[i]; }
  a.l = FIRST ? ps : a.l + ps;
  bf16x8 pf[2];
#pragma unroll
  for (int s = 0; s < 2; ++s)
    pf[s] = __builtin_bit_cast(bf16x8, u32x4{cvtpk(sc[8 * s], sc[8 * s + 1]), cvtpk(sc[8 * s + 2], sc[8 * s + 3]), cvtpk(sc[8 * s + 4], sc[8 * s + 5]), cvtpk(sc[8 * s + 6], sc[8 * s + 7])});
  asm volatile("" : "+v"(pf[0]), "+v"(pf[1]), "+v"(a.l));
  if (!FIRST) take_v<MORE>(a, t, vf);
#pragma unroll
  for (int s = 0; s < 2; ++s)
#pragma unroll
    for (int cb = 0; cb < 4; ++cb) a.o[cb] = __builtin_amdgcn_mfma_f32_32x32x16_bf16(vf[cb][s], pf[s], FIRST && s == 0 ? f32x16{} : a.o[cb], 0, 0, 0);
}

__global__ __launch_bounds__(256, 2) void fused_kernel(const void* __restrict__ xA, const void* __restrict__ wqA, const void* __restrict__ kA, const void* __restrict__ vA,
                                                       const void* __restrict__ xB, const void* __restrict__ wqB, const void* __restrict__ kB, const void* __restrict__ vB,
                                                       const void* __restrict__ woA, const void* __restrict__ woB, char* ws, __hip_bfloat16* __restrict__ out) {
  extern __shared__ __attribute__((aligned(16))) char smem_[];
  LAS char* smem = (LAS char*)smem_;
  const unsigned lds0 = (unsigned)(uintptr_t)smem;
  const int lane = threadIdx.x & 63, r32 = lane & 31, hi = lane >> 5;
  const int wave = rfl(threadIdx.x >> 6), br = wave >> 1, kh = wave & 1;
  const unsigned ring = wave * 32768u;
  const int g8 = blockIdx.x & 7, jj = blockIdx.x >> 3, b = jj >> 2, head = 4 * g8 + (jj & 3);
  Att a;
  bf16x8 kf0[8], vf0[4][2];
  {
    const int cc = lane & 15, g = lane >> 4;
    unsigned wvo = g * 65536 + 16 * cc, xvo = hi * 1024 + 16 * (r32 ^ hi);
    const unsigned ximg = ring + 16384;
    asm volatile("" : "+v"(wvo), "+v"(xvo));
    SBAR();
    const rsrc_t srdX = mk_rsrc((const char*)(br ? xB : xA) + (size_t)b * (SQ * DM * 2), SQ * DM * 2);
    const rsrc_t srdW = mk_rsrc(br ? wqB : wqA, DM * NH * DH * 2);
    u32x4 wr[8][8];
    f32x4 acc[8][2];
#define XISSUE(I0, I1) _Pragma("unroll") for (int i = (I0); i < (I1); ++i) { const unsigned p = (unsigned)(i + 4 * jj) & 15u; \
      dma_piece(lds0 + ximg + 1024u * p, srdX, xvo ^ (32u * (p & 7u)), p * 2048u + kh * 512u); }
#define QISSUE(ks) do { const unsigned kk = (unsigned)(kh * 8 + (((ks) + b) & 7)); \
      _Pragma("unroll") for (int i = 0; i < 8; ++i) wr[ks][i] = bload(srdW, wvo, (kk * 32 + i) * 8192 + head * 256); } while (0)
    XISSUE(0, 8); QISSUE(0); XISSUE(8, 16);
#pragma unroll
    for (int ks = 1; ks < QBUF; ++ks) QISSUE(ks);
    {
      a.ldsK = lds0 + ring; a.nt = br; a.kh = kh;
      const size_t kvoff = (size_t)(br ? b * NH + head : b * HKV + g8) * (SK * DH * 2);
      a.srdK = mk_rsrc((const char*)(br ? kB : kA) + kvoff, SK * DH * 2);
      a.srdV = mk_rsrc((const char*)(br ? vB : vA) + kvoff, SK * DH * 2);
#pragma unroll
      for (int i = 0; i < 4; ++i) a.vo[i] = g * 256 + 16 * (cc ^ ((g << 2) | i));
      asm volatile("" :: "s"(woA), "s"(woB), "s"(ws), "s"(out));
      const unsigned kaddr = ring + 256 * r32 + 16 * (hi ^ (((r32 & 3) << 2) | ((r32 >> 2) & 3)));
      const int blk = (lane >> 4) & 1, q4 = (lane & 15) >> 2, p4 = lane & 3;
      const unsigned vaddr = (ring + 8192 + 256 * (4 * hi + q4) + 64 * q4 + 16 * ((2 * blk + (p4 & 1)) ^ hi) + 8 * (p4 >> 1)) ^ (kh * 128u);
#pragma unroll
      for (int s = 0; s < 8; ++s) { a.ka[s] = lds0 + (kaddr ^ (32u * s)); a.va[s] = lds0 + (vaddr ^ (32u * s)); }
      asm volatile("" : "+v"(a.ka[0]), "+v"(a.ka[1]), "+v"(a.ka[2]), "+v"(a.ka[3]), "+v"(a.ka[4]), "+v"(a.ka[5]), "+v"(a.ka[6]), "+v"(a.ka[7]),
                        "+v"(a.va[0]), "+v"(a.va[1]), "+v"(a.va[2]), "+v"(a.va[3]), "+v"(a.va[4]), "+v"(a.va[5]), "+v"(a.va[6]), "+v"(a.va[7]));
    }
#pragma unroll
    for (int ks = 0; ks < 8; ++ks) {
      SBAR();
      const int n = ((ks + QBUF - 1 < 7 ? ks + QBUF - 1 : 7) - ks) * 8 + (ks > 4 ? 8 : 0) + (ks > 6 ? 8 : 0);
      asm volatile("s_waitcnt vmcnt(%8)" : "+v"(wr[ks][0]), "+v"(wr[ks][1]), "+v"(wr[ks][2]), "+v"(wr[ks][3]), "+v"(wr[ks][4]), "+v"(wr[ks][5]), "+v"(wr[ks][6]), "+v"(wr[ks][7]) : "n"(n) : "memory");
      if (ks + QBUF < 8) QISSUE((ks + QBUF) & 7);
      SBAR();
      bf16x8 xf[2];
      const unsigned ch = 4u * ((ks + b) & 7) + g;
#pragma unroll
      for (int qb = 0; qb < 2; ++qb) xf[qb] = *(LAS bf16x8*)(smem + ximg + (cc + 16 * (qb ^ kh)) * 512 + 16 * (ch ^ cc));
#pragma unroll
      for (int j = 0; j < 8; ++j) {
        u32x4 f;
#pragma unroll
        for (int m = 0; m < 4; ++m) f[m] = __builtin_amdgcn_perm(wr[ks][2 * m + 1][j >> 1], wr[ks][2 * m][j >> 1], (j & 1) ? 0x07060302u : 0x05040100u);
#pragma unroll
        for (int qb = 0; qb < 2; ++qb)
          acc[j][qb] = __builtin_amdgcn_mfma_f32_16x16x32_bf16(__builtin_bit_cast(bf16x8, f), xf[qb], ks ? acc[j][qb] : f32x4{0.f, 0.f, 0.f, 0.f}, 0, 0, 0);
      }
      __builtin_amdgcn_sched_group_barrier(0x100, 2, 0);
      __builtin_amdgcn_sched_group_barrier(0x2, 8, 0);
#pragma unroll
      for (int j = 0; j < 12; ++j) { __builtin_amdgcn_sched_group_barrier(0x8, 1, 0); __builtin_amdgcn_sched_group_barrier(0x2, 2, 0); }
      __builtin_amdgcn_sched_group_barrier(0x8, 4, 0);
      SBAR();
      if (ks == 4) dma_tile(a.nt, a.ldsK, a.srdK, kh * 8192u, a.vo);
      if (ks == 6) dma_tile(a.nt, a.ldsK + 8192u, a.srdV, kh * 8192u, a.vo);
    }
#undef XISSUE
#undef QISSUE
    LAS f32x4* mine = (LAS f32x4*)(smem + SCR + wave * 8192) + lane;
#pragma unroll
    for (int j = 0; j < 8; ++j) mine[j * 64] = acc[j][1];
    take_k<true>(a, 0, kf0);
    bar();
    LAS f32x4* other = (LAS f32x4*)(smem + SCR + (wave ^ 1) * 8192) + lane;
#pragma unroll
    for (int j = 0; j < 8; ++j) acc[j][0] += other[j * 64];
    LAS char* qimg = smem + (wave & 2) * 32768 + 16384;
#pragma unroll
    for (int e = 0; e < 4; ++e)
      *(LAS u32x4*)(qimg + (cc + 16 * kh) * 256 + 16 * ((4 * g + e) ^ cc)) =
          u32x4{cvtpk(acc[0][0][e], acc[1][0][e]), cvtpk(acc[2][0][e], acc[3][0][e]), cvtpk(acc[4][0][e], acc[5][0][e]), cvtpk(acc[6][0][e], acc[7][0][e])};
    take_v<true>(a, 0, vf0);
    bar();
#pragma unroll
    for (int s = 0; s < 8; ++s) a.qf[s] = *(LAS bf16x8*)(qimg + r32 * 256 + 16 * ((2 * s + hi) ^ (lane & 15)));
  }

  attn_tile<true, true>(a, 0, kf0, vf0);
#pragma unroll 1
  for (int t = 1; t < NT - 1; ++t) { bf16x8 kf[8], vf[4][2]; attn_tile<false, true>(a, t, kf, vf); }
  { bf16x8 kf[8], vf[4][2]; attn_tile<false, false>(a, NT - 1, kf, vf); }

  unsigned long long gen = __hip_atomic_load((GAS unsigned long long*)(ws + WS_GEN), RLX_AGENT);
  GAS unsigned* flags = (GAS unsigned*)(ws + WS_FLAG);

  {
    { auto rr = __builtin_amdgcn_permlane32_swap(__float_as_uint(a.l), __float_as_uint(a.l), false, false); a.l = __uint_as_float(rr[0]) + __uint_as_float(rr[1]); }
    LAS f32x4* mine = (LAS f32x4*)(smem + SCR + wave * 8192) + lane;
    LAS f32x4* other = (LAS f32x4*)(smem + SCR + (wave ^ 1) * 8192) + lane;
    *(LAS f32x2*)(smem + ring + lane * 8) = f32x2{a.m, a.l};
#pragma unroll
    for (int u = 0; u < 8; ++u) mine[u * 64] = f32x4{a.o[2 + (u >> 2)][4 * (u & 3)], a.o[2 + (u >> 2)][4 * (u & 3) + 1], a.o[2 + (u >> 2)][4 * (u & 3) + 2], a.o[2 + (u >> 2)][4 * (u & 3) + 3]};
    const rsrc_t srdOw = mk_rsrc(ws + WS_O + (size_t)((br * NB + b) * NH + head) * 8192 + kh * 4096, 4096);
    bar();
    const f32x2 v = *(LAS f32x2*)(smem + (ring ^ 32768u) + lane * 8);
    f32x4 p[8];
#pragma unroll
    for (int u = 0; u < 8; ++u) p[u] = other[u * 64];
    SBAR();
    const float ms = fmaxf(a.m, v[0]), e0 = __builtin_amdgcn_exp2f((a.m - ms) * SC2), e1 = __builtin_amdgcn_exp2f((v[0] - ms) * SC2);
    const float inv = __builtin_amdgcn_rcpf(fmaf(e0, a.l, e1 * v[1])), f0 = e0 * inv, f1 = e1 * inv;
#pragma unroll
    for (int u = 0; u < 4; ++u) {
      const f32x4 a0 = p[2 * u] * f1, a1 = p[2 * u + 1] * f1;
      const int cb = u >> 1, r0 = 8 * (u & 1);
      const u32x4 w = {cvtpk(fmaf(a.o[cb][r0], f0, a0[0]), fmaf(a.o[cb][r0 + 1], f0, a0[1])), cvtpk(fmaf(a.o[cb][r0 + 2], f0, a0[2]), fmaf(a.o[cb][r0 + 3], f0, a0[3])),
                       cvtpk(fmaf(a.o[cb][r0 + 4], f0, a1[0]), fmaf(a.o[cb][r0 + 5], f0, a1[1])), cvtpk(fmaf(a.o[cb][r0 + 6], f0, a1[2]), fmaf(a.o[cb][r0 + 7], f0, a1[3]))};
      __builtin_amdgcn_raw_buffer_store_b128(w, srdOw, lane * 16 + u * 1024, 0, 16);
    }
  }
  SBAR();
  const int obr = g8 & 1, okq = (g8 >> 1) ^ 1, ob = 2 * (jj >> 3), ocb = jj & 7, h0 = okq * 8 + ((wave + ocb + (ob >> 1)) & 3) * 2, orot = (ob + ocb) & 7;
  const rsrc_t srdWo = mk_rsrc(obr ? woB : woA, NH * DH * DM * 2);
  const rsrc_t srdO = mk_rsrc(ws + WS_O + (size_t)((obr * NB + ob) * NH + h0) * 8192, NH * 8192 + 2 * 8192);
  unsigned wvo = (lane >> 3) * 1024 + 16 * ((lane & 7) ^ (((lane >> 4) & 1) << 2));
  unsigned waddr = (ring + 128 * (8 * hi + ((lane & 15) >> 2)) + 64 * ((lane & 15) >> 3) + 32 * ((lane >> 4) & 1) + 8 * (lane & 3)) ^ (16u * wave) ^ (64u * (okq & 1));
  GAS unsigned* fp = flags + ((obr * NB + ob + ((lane >> 1) & 1)) * NH + h0 + (lane & 1)) * FS;
  GAS unsigned* myflag = threadIdx.x < 2 ? flags + ((threadIdx.x * NB + b) * NH + head) * FS : (GAS unsigned*)(ws + WS_SEEN) + blockIdx.x;
  asm volatile("" : "+v"(wvo), "+v"(waddr), "+v"(fp), "+v"(myflag) : "s"(srdWo), "s"(srdO));
  vmwait<0>();
  asm volatile("" : "+v"(gen));
  const unsigned long long tag64 = (((unsigned long long)rfl((unsigned)(gen >> 32)) << 32) | rfl((unsigned)gen)) + 1ull;
  const unsigned tag = (unsigned)tag64;
  bar();
  if (threadIdx.x < 3) __hip_atomic_store(myflag, tag, RLX_AGENT);
  f32x16 acc[4] = {};
  {
#define DMA_W(I0, I1) _Pragma("unroll") for (int i = (I0); i < (I1); ++i)        \
      dma_piece(lds0 + ring + 1024u * i, srdWo, wvo, (unsigned)(h0 * 128 + (((i >> 2) + orot) & 7) * 32 + (i & 3) * 8) * 1024u + ocb * 128u)
#define RD_W(S0, S1) _Pragma("unroll") for (int s = (S0); s < (S1); ++s) _Pragma("unroll") for (int nb = 0; nb < 2; ++nb) { \
      const bf16x4 lo = __builtin_amdgcn_ds_read_tr16_b64_v4bf16((LAS bf16x4*)(smem + (waddr ^ (64u * nb)) + 2048u * s)); \
      const bf16x4 up = __builtin_amdgcn_ds_read_tr16_b64_v4bf16((LAS bf16x4*)(smem + (waddr ^ (64u * nb)) + 2048u * s + 512u)); \
      wf[s][nb] = __builtin_shufflevector(lo, up, 0, 1, 2, 3, 4, 5, 6, 7); }
#define O_ISSUE(S0, S1) _Pragma("unroll") for (int s = (S0); s < (S1); ++s) _Pragma("unroll") for (int bi = 0; bi < 2; ++bi) \
      of[s][bi] = bload_sc1(srdO, lane * 16, (unsigned)((s + 2 * orot) & 15) * 1024u + (bi ^ (okq >> 1)) * (NH * 8192u))
#define O_WAIT(S, N) asm volatile("s_waitcnt vmcnt(%4)" : "+v"(of[S][0]), "+v"(of[S][1]), "+v"(of[(S) + 1][0]), "+v"(of[(S) + 1][1]) : "n"(N) : "memory")
#define O_MFMA(S0) _Pragma("unroll") for (int s = (S0); s < (S0) + 2; ++s) _Pragma("unroll") for (int j = 0; j < 4; ++j) \
      acc[j] = __builtin_amdgcn_mfma_f32_32x32x16_bf16(wf[s][j & 1], __builtin_bit_cast(bf16x8, of[s][j >> 1]), acc[j], 0, 0, 0)
    unsigned f0;
    asm volatile("global_load_dword %0, %1, off sc1" : "=v"(f0) : "v"(fp) : "memory");
    DMA_W(0, 8);
    asm volatile("s_waitcnt vmcnt(8)" : "+v"(f0) :: "memory");
    if (__all(f0 == tag)) {
      u32x4 of[16][2]; bf16x8 wf[16][2];
      O_ISSUE(0, 4); DMA_W(8, 16);
#pragma unroll
      for (int s0 = 0; s0 < 16; s0 += 2) {
        SBAR();
        O_WAIT(s0, s0 == 2 ? 16 : s0 <= 8 ? 12 : 28 - 2 * s0);
        RD_W(s0, s0 + 2);
        if (s0 + 4 < 16) O_ISSUE(s0 + 4, s0 + 6);
        if (s0 < 8) DMA_W(2 * s0 + 16, 2 * s0 + 20);
        SBAR();
        O_MFMA(s0);
      }
    } else {
      unsigned f1;
      DMA_W(8, 24);
      asm volatile("global_load_dword %0, %1, off sc1" : "=v"(f1) : "v"(fp) : "memory");
      DMA_W(24, 32);
      asm volatile("s_waitcnt vmcnt(%1)" : "+v"(f1) : "n"(8) : "memory");
      if (!__all(f1 == tag)) {
#pragma unroll 1
        for (unsigned spins = 0; spins < SPIN_CAP; ++spins) {
          if (__all(__hip_atomic_load(fp, RLX_AGENT) == tag)) break;
          __builtin_amdgcn_s_sleep(1);
        }
      }
      asm volatile("" ::: "memory");
      {
        u32x4 of[16][2]; bf16x8 wf[16][2];
        O_ISSUE(0, 12);
        vmwait<24>();
        RD_W(0, 4);
#pragma unroll
        for (int s0 = 0; s0 < 16; s0 += 2) {
          SBAR();
          O_WAIT(s0, s0 < 4 ? 20 : 28 - 2 * s0);
          if (s0 < 4) O_ISSUE(12 + s0, 14 + s0);
          if (s0 + 4 < 16) RD_W(s0 + 4, s0 + 6);
          SBAR();
          O_MFMA(s0);
        }
      }
    }
#undef DMA_W
#undef RD_W
#undef O_ISSUE
#undef O_WAIT
#undef O_MFMA
  }
#pragma unroll
  for (int u = 0; u < 16; ++u)
    if (u & 3) *((LAS f32x4*)(smem + ring) + u * 64 + lane) = f32x4{acc[u >> 2][4 * (u & 3)], acc[u >> 2][4 * (u & 3) + 1], acc[u >> 2][4 * (u & 3) + 2], acc[u >> 2][4 * (u & 3) + 3]};
  {
    const unsigned t0 = tag & 0x3fffffu, t1 = ((unsigned)(tag64 >> 22) ^ tag) & 0x3fffffu;
    const rsrc_t srdB = mk_rsrc(ws + WS_BOX, 256 * 16384), srdOut = mk_rsrc(out, NB * SQ * 2048);
    unsigned bvo = (jj * 8 + obr * 4 + okq) * 16384 + threadIdx.x * 16, ovo = r32 * 2048 + 8 * hi, ra[3];
    const unsigned oso = (ob + (okq >> 1)) * SQ * 2048 + obr * 1024 + ocb * 128 + (okq & 1) * 64 + 16 * wave;
#pragma unroll
    for (int x = 1; x < 4; ++x) ra[x - 1] = lds0 + (ring ^ (x * 32768u)) + x * 1024 + lane * 16;
    asm volatile("" : "+v"(bvo), "+v"(ovo), "+v"(ra[0]), "+v"(ra[1]), "+v"(ra[2]) : "s"(srdB), "s"(srdOut), "s"(oso), "s"(t1));
    bar();
    f32x4 p[4][3], s;
#pragma unroll
    for (int r = 3; r >= 0; --r)
#pragma unroll
      for (int x = 0; x < 3; ++x) p[r][x] = *((const LAS f32x4*)(uintptr_t)ra[x] + r * 256);
    SBAR();
#pragma unroll
    for (int r = 3; r >= 0; --r) {
      s = f32x4{acc[r][0], acc[r][1], acc[r][2], acc[r][3]} + p[r][0] + p[r][1] + p[r][2];
      if (r) {
        const u32x4 u = __builtin_bit_cast(u32x4, s) + 0x400u;
        const u32x4 w = {__builtin_amdgcn_alignbit(u[1] >> 11, u[0], 11), __builtin_amdgcn_alignbit(t0, u[1], 22), __builtin_amdgcn_alignbit(u[3] >> 11, u[2], 11), __builtin_amdgcn_alignbit(t1, u[3], 22)};
        __builtin_amdgcn_raw_buffer_store_b128(w, srdB, bvo ^ (r * 20480u), 0, 16);
      }
    }
    u32x4 g[3];
#pragma unroll
    for (int i = 0; i < 3; ++i) g[i] = __builtin_amdgcn_raw_buffer_load_b128(srdB, bvo, (1 + i) * 4096, 16);
    asm volatile("" : "+v"(s));
#pragma unroll 1
    for (unsigned spins = 0; spins < SPIN_CAP; ++spins) {
      unsigned bad = 0;
#pragma unroll
      for (int i = 0; i < 3; ++i) bad |= (g[i][1] ^ (t0 << 10)) | (g[i][3] ^ (t1 << 10));
      if (__all(bad < 1024u)) break;
      asm volatile("s_sleep 1" ::: "memory");
#pragma unroll
      for (int i = 0; i < 3; ++i) g[i] = __builtin_amdgcn_raw_buffer_load_b128(srdB, bvo, (1 + i) * 4096, 16);
    }
#pragma unroll
    for (int i = 0; i < 3; ++i)
      s += __builtin_bit_cast(f32x4, u32x4{g[i][0], __builtin_amdgcn_alignbit(g[i][1], g[i][0], 21), g[i][2], __builtin_amdgcn_alignbit(g[i][3], g[i][2], 21)} << 11);
    __builtin_amdgcn_raw_buffer_store_b64(u32x2{cvtpk(s[0], s[1]), cvtpk(s[2], s[3])}, srdOut, ovo, oso, 16);
  }
  if (blockIdx.x == 0 && wave == 0) {
    const rsrc_t srdS = mk_rsrc(ws + WS_SEEN, 1024);
#pragma unroll 1
    for (unsigned spins = 0; spins < SPIN_CAP; ++spins) {
      const u32x4 v = __builtin_amdgcn_raw_buffer_load_b128(srdS, lane * 16, 0, 16);
      if (__all(((v[0] ^ tag) | (v[1] ^ tag) | (v[2] ^ tag) | (v[3] ^ tag)) == 0)) break;
      asm volatile("s_sleep 1" ::: "memory");
    }
    if (lane == 0) __hip_atomic_store((GAS unsigned long long*)(ws + WS_GEN), tag64, RLX_AGENT);
  }
}

extern "C" void kernel_launch(void* const* d_in, const int* in_sizes, int n_in, void* d_out, int out_size, void* d_ws, size_t ws_size, hipStream_t stream) {
  (void)in_sizes; (void)n_in; (void)out_size; (void)ws_size;
  (void)hipFuncSetAttribute((const void*)fused_kernel, hipFuncAttributeMaxDynamicSharedMemorySize, LDS_BYTES);
  hipLaunchKernelGGL(fused_kernel, dim3(256), dim3(256), LDS_BYTES, stream, d_in[0], d_in[3], d_in[1], d_in[2], d_in[5], d_in[8], d_in[6], d_in[7], d_in[4], d_in[9],
                     (char*)d_ws, (__hip_bfloat16*)d_out);
}
```
